```python
import torch

def _softmax(x, axis=-1):
    return torch.softmax(x, dim=axis)
def _gelu(x):
    return torch.nn.functional.gelu(x, approximate="tanh")
def _silu(x):
    return torch.nn.functional.silu(x)
def _sigmoid(x):
    return torch.sigmoid(x)
def _layernorm(x, axis=-1, eps=1e-5):
    m = x.mean(dim=axis, keepdim=True)
    v = x.var(dim=axis, keepdim=True, unbiased=False)
    return (x - m) / torch.sqrt(v + eps)
def _scatter_add(tgt, idx, src, axis=0):
    return tgt.clone().index_add_(axis, idx, src)
def _segment_sum(src, seg, n):
    out = torch.zeros((n,) + src.shape[1:], dtype=src.dtype)
    return out.index_add_(0, seg, src)
def _pad(x, pw):
    flat = tuple(v for pair in reversed(pw) for v in pair)
    return torch.nn.functional.pad(x, flat)

kernel_name = 'standalone__softmax_full_bidir__sampling'

def setup_inputs():

    _w = (lambda a: (lambda t: t.to(torch.bfloat16) if t.is_floating_point() else t)(torch.as_tensor(a)))
    B, H, Sq, Skv, Dh = 8, 16, 16, 2048, 256
    return {
        "q": _w((lambda *s: torch.randn(*s, dtype=torch.bfloat16) * 0.8)(B, H, Sq, Dh)),
        "k": _w((lambda *s: torch.randn(*s, dtype=torch.bfloat16) * 0.8)(B, H, Skv, Dh)),
        "v": _w((lambda *s: torch.randn(*s, dtype=torch.bfloat16) * 0.05)(B, H, Skv, Dh)),
    }

def reference(q, k, v):
    scale = q.shape[-1] ** -0.5
    scores = torch.einsum('bhqd,bhkd->bhqk', q, k) * scale
    attn = _softmax(scores, axis=-1)
    return torch.einsum('bhqk,bhkd->bhqd', attn, v)
```

```cpp
#include <hip/hip_runtime.h>
#include <hip/hip_bf16.h>

typedef unsigned v4u __attribute__((ext_vector_type(4)));
typedef unsigned v2u __attribute__((ext_vector_type(2)));
typedef float f32x4 __attribute__((ext_vector_type(4)));
typedef short bf16x8 __attribute__((ext_vector_type(8)));
typedef short bf16x4 __attribute__((ext_vector_type(4)));
typedef __bf16 bf2 __attribute__((ext_vector_type(2)));

constexpr int NHEAD = 128, SQ = 16, SKV = 2048, DH = 256;
constexpr int NW = 4;
constexpr int TK = 16;
constexpr int NT = SKV / TK;
constexpr int AUX_NT = 2;
constexpr int OSTR = 132;
constexpr float C2 = 0.0625f * 1.4426950408889634f;

__device__ __forceinline__ f32x4 mfma32(v4u a, v4u b, f32x4 c) {
  return __builtin_amdgcn_mfma_f32_16x16x32_bf16(__builtin_bit_cast(bf16x8, a), __builtin_bit_cast(bf16x8, b), c, 0, 0, 0);
}
__device__ __forceinline__ f32x4 mfma16(v2u a, v2u b, f32x4 c) {
  return __builtin_amdgcn_mfma_f32_16x16x16bf16_1k(__builtin_bit_cast(bf16x4, a), __builtin_bit_cast(bf16x4, b), c, 0, 0, 0);
}
__device__ __forceinline__ unsigned pack_bf16(float lo, float hi) {
  bf2 r = {(__bf16)lo, (__bf16)hi};
  return __builtin_bit_cast(unsigned, r);
}
__device__ __forceinline__ float xg_max(float x) {
  auto s = __builtin_amdgcn_permlane16_swap(__float_as_uint(x), __float_as_uint(x), false, false);
  x = fmaxf(__uint_as_float(s[0]), __uint_as_float(s[1]));
  auto t = __builtin_amdgcn_permlane32_swap(__float_as_uint(x), __float_as_uint(x), false, false);
  return fmaxf(__uint_as_float(t[0]), __uint_as_float(t[1]));
}
__device__ __forceinline__ float xg_sum(float x) {
  auto s = __builtin_amdgcn_permlane16_swap(__float_as_uint(x), __float_as_uint(x), false, false);
  x = __uint_as_float(s[0]) + __uint_as_float(s[1]);
  auto t = __builtin_amdgcn_permlane32_swap(__float_as_uint(x), __float_as_uint(x), false, false);
  return __uint_as_float(t[0]) + __uint_as_float(t[1]);
}

__global__ __launch_bounds__(NW * 64, 1) void attn_decode_kernel(const void* __restrict__ Q, const void* __restrict__ K,
                                                                 const void* __restrict__ V, void* __restrict__ O) {
  __shared__ __attribute__((aligned(16))) float lds[NW * SQ * OSTR + NW * SQ * 2];
  const int tid = threadIdx.x, lane = tid & 63, w = __builtin_amdgcn_readfirstlane(tid >> 6);
  const int g = lane >> 4, r = lane & 15;
  const int bid = blockIdx.x;
  const int head = (bid & 7) + 8 * (bid >> 4), half = (bid >> 3) & 1;

  const __amdgpu_buffer_rsrc_t rq = __builtin_amdgcn_make_buffer_rsrc((char*)Q + (size_t)head * (SQ * DH * 2), 0, SQ * DH * 2, 0x00020000);
  const __amdgpu_buffer_rsrc_t rk = __builtin_amdgcn_make_buffer_rsrc((char*)K + (size_t)head * (SKV * DH * 2), 0, SKV * DH * 2, 0x00020000);
  const __amdgpu_buffer_rsrc_t rv = __builtin_amdgcn_make_buffer_rsrc((char*)V + (size_t)head * (SKV * DH * 2), 0, SKV * DH * 2, 0x00020000);

  v4u qf[2][4];
#pragma unroll
  for (int h = 0; h < 2; ++h)
#pragma unroll
    for (int b = 0; b < 4; ++b) qf[h][b] = __builtin_amdgcn_raw_buffer_load_b128(rq, r * 512 + h * 256 + g * 64 + b * 16, 0, 0);

  const int klane = ((lane >> 2) & 3) * 512 + g * 64 + (lane & 3) * 16;
  const int vlane = g * 512 + half * 256 + r * 16;
  int base = w * (TK * 512);
  v4u kf[4][2], vf[4];
#pragma unroll
  for (int A = 0; A < 4; ++A)
#pragma unroll
    for (int h = 0; h < 2; ++h) kf[A][h] = __builtin_amdgcn_raw_buffer_load_b128(rk, base + klane + A * 2048 + h * 256, 0, AUX_NT);
#pragma unroll
  for (int i = 0; i < 4; ++i) vf[i] = __builtin_amdgcn_raw_buffer_load_b128(rv, base + vlane + i * 2048, 0, AUX_NT);
  __builtin_amdgcn_sched_barrier(0);

  f32x4 acc[8];
#pragma unroll
  for (int e = 0; e < 8; ++e) acc[e] = f32x4{0.f, 0.f, 0.f, 0.f};
  float mc = -__builtin_inff(), l = 0.f;

  for (int it = 0; it < NT / NW; ++it) {
    base += NW * TK * 512;
    const int kb = base + klane, vb = base + vlane;
    float s[4];
#pragma unroll
    for (int A = 0; A < 4; ++A) {
      f32x4 sa[4];
#pragma unroll
      for (int b = 0; b < 4; ++b) sa[b] = mfma32(kf[A][0], qf[0][b], f32x4{0.f, 0.f, 0.f, 0.f});
      kf[A][0] = __builtin_amdgcn_raw_buffer_load_b128(rk, kb + A * 2048, 0, AUX_NT);
      __builtin_amdgcn_sched_barrier(0);
#pragma unroll
      for (int b = 0; b < 4; ++b) sa[b] = mfma32(kf[A][1], qf[1][b], sa[b]);
      kf[A][1] = __builtin_amdgcn_raw_buffer_load_b128(rk, kb + A * 2048 + 256, 0, AUX_NT);
      __builtin_amdgcn_sched_barrier(0);
      s[A] = (sa[0][0] + sa[1][1]) + (sa[2][2] + sa[3][3]);
    }
    const float mx = xg_max(fmaxf(fmaxf(s[0], s[1]), fmaxf(s[2], s[3])));
    const float mn = fmaxf(mc, mx * C2);
    const float alpha = __builtin_amdgcn_exp2f(mc - mn);
    mc = mn;
    float p[4];
#pragma unroll
    for (int A = 0; A < 4; ++A) p[A] = __builtin_amdgcn_exp2f(__builtin_fmaf(s[A], C2, -mn));
    l = __builtin_fmaf(l, alpha, (p[0] + p[1]) + (p[2] + p[3]));
    const v2u pb = {pack_bf16(p[0], p[1]), pack_bf16(p[2], p[3])};
    if (__any(alpha != 1.f)) {
#pragma unroll
      for (int e = 0; e < 8; ++e) acc[e] *= alpha;
    }
    v2u va[8];
#pragma unroll
    for (int k = 0; k < 4; ++k) {
      va[2 * k][0] = __builtin_amdgcn_perm(vf[1][k], vf[0][k], 0x05040100u);
      va[2 * k + 1][0] = __builtin_amdgcn_perm(vf[1][k], vf[0][k], 0x07060302u);
    }
    vf[0] = __builtin_amdgcn_raw_buffer_load_b128(rv, vb, 0, AUX_NT);
    vf[1] = __builtin_amdgcn_raw_buffer_load_b128(rv, vb + 2048, 0, AUX_NT);
    __builtin_amdgcn_sched_barrier(0);
#pragma unroll
    for (int k = 0; k < 4; ++k) {
      va[2 * k][1] = __builtin_amdgcn_perm(vf[3][k], vf[2][k], 0x05040100u);
      va[2 * k + 1][1] = __builtin_amdgcn_perm(vf[3][k], vf[2][k], 0x07060302u);
    }
    vf[2] = __builtin_amdgcn_raw_buffer_load_b128(rv, vb + 4096, 0, AUX_NT);
    vf[3] = __builtin_amdgcn_raw_buffer_load_b128(rv, vb + 6144, 0, AUX_NT);
    __builtin_amdgcn_sched_barrier(0);
#pragma unroll
    for (int e = 0; e < 8; ++e) acc[e] = mfma16(va[e], pb, acc[e]);
    __builtin_amdgcn_s_barrier();
  }

  l = xg_sum(l);
  float* ob = lds + (w * SQ + r) * OSTR + 32 * g;
#pragma unroll
  for (int rho = 0; rho < 4; ++rho) {
    *(f32x4*)(ob + 8 * rho) = f32x4{acc[0][rho], acc[1][rho], acc[2][rho], acc[3][rho]};
    *(f32x4*)(ob + 8 * rho + 4) = f32x4{acc[4][rho], acc[5][rho], acc[6][rho], acc[7][rho]};
  }
  float* ml = lds + NW * SQ * OSTR;
  if (g == 0) { ml[(w * SQ + r) * 2] = mc; ml[(w * SQ + r) * 2 + 1] = l; }
  __syncthreads();
  {
    const int q = tid >> 4, c = tid & 15;
    float M = ml[q * 2];
#pragma unroll
    for (int ww = 1; ww < NW; ++ww) M = fmaxf(M, ml[(ww * SQ + q) * 2]);
    float L = 0.f;
    f32x4 o0 = {0.f, 0.f, 0.f, 0.f}, o1 = {0.f, 0.f, 0.f, 0.f};
#pragma unroll
    for (int ww = 0; ww < NW; ++ww) {
      const float f = __builtin_amdgcn_exp2f(ml[(ww * SQ + q) * 2] - M);
      L = __builtin_fmaf(f, ml[(ww * SQ + q) * 2 + 1], L);
      const float* src = lds + (ww * SQ + q) * OSTR + 8 * c;
      o0 += f * *(const f32x4*)src;
      o1 += f * *(const f32x4*)(src + 4);
    }
    const float inv = 1.f / L;
    o0 *= inv; o1 *= inv;
    v4u o = {pack_bf16(o0[0], o0[1]), pack_bf16(o0[2], o0[3]), pack_bf16(o1[0], o1[1]), pack_bf16(o1[2], o1[3])};
    *(v4u*)((char*)O + ((size_t)head * SQ + q) * (DH * 2) + half * 256 + c * 16) = o;
  }
}

extern "C" void kernel_launch(void* const* d_in, const int* in_sizes, int n_in, void* d_out, int out_size, void* d_ws,
                              size_t ws_size, hipStream_t stream) {
  attn_decode_kernel<<<2 * NHEAD, NW * 64, 0, stream>>>(d_in[0], d_in[1], d_in[2], d_out);
}
```

```cpp
#include <hip/hip_runtime.h>
#include <hip/hip_bf16.h>

typedef unsigned v4u __attribute__((ext_vector_type(4)));
typedef unsigned v2u __attribute__((ext_vector_type(2)));
typedef float f32x4 __attribute__((ext_vector_type(4)));
typedef short bf16x8 __attribute__((ext_vector_type(8)));
typedef short bf16x4 __attribute__((ext_vector_type(4)));
typedef __bf16 bf2 __attribute__((ext_vector_type(2)));

constexpr int NHEAD = 128, SQ = 16, SKV = 2048, DH = 256;
constexpr int NW = 4;
constexpr int TK = 16;
constexpr int NT = SKV / TK;
#define AUXK 0
#define AUXV 2
constexpr int OSTR = 132;
constexpr float C2 = 0.0625f * 1.4426950408889634f;

__device__ __forceinline__ f32x4 mfma32(v4u a, v4u b, f32x4 c) {
  return __builtin_amdgcn_mfma_f32_16x16x32_bf16(__builtin_bit_cast(bf16x8, a), __builtin_bit_cast(bf16x8, b), c, 0, 0, 0);
}
__device__ __forceinline__ f32x4 mfma16(v2u a, v2u b, f32x4 c) {
  return __builtin_amdgcn_mfma_f32_16x16x16bf16_1k(__builtin_bit_cast(bf16x4, a), __builtin_bit_cast(bf16x4, b), c, 0, 0, 0);
}
__device__ __forceinline__ unsigned pack_bf16(float lo, float hi) {
  bf2 r = {(__bf16)lo, (__bf16)hi};
  return __builtin_bit_cast(unsigned, r);
}
__device__ __forceinline__ float xg_max(float x) {
  auto s = __builtin_amdgcn_permlane16_swap(__float_as_uint(x), __float_as_uint(x), false, false);
  x = fmaxf(__uint_as_float(s[0]), __uint_as_float(s[1]));
  auto t = __builtin_amdgcn_permlane32_swap(__float_as_uint(x), __float_as_uint(x), false, false);
  return fmaxf(__uint_as_float(t[0]), __uint_as_float(t[1]));
}
__device__ __forceinline__ float xg_sum(float x) {
  auto s = __builtin_amdgcn_permlane16_swap(__float_as_uint(x), __float_as_uint(x), false, false);
  x = __uint_as_float(s[0]) + __uint_as_float(s[1]);
  auto t = __builtin_amdgcn_permlane32_swap(__float_as_uint(x), __float_as_uint(x), false, false);
  return __uint_as_float(t[0]) + __uint_as_float(t[1]);
}

__global__ __launch_bounds__(NW * 64, 2) void attn_decode_kernel(const void* __restrict__ Q, const void* __restrict__ K,
                                                                 const void* __restrict__ V, void* __restrict__ O) {
  __shared__ __attribute__((aligned(16))) float lds[NW * SQ * OSTR + NW * SQ * 2];
  const int tid = threadIdx.x, lane = tid & 63, w = __builtin_amdgcn_readfirstlane(tid >> 6);
  const int g = lane >> 4, r = lane & 15;
  const int bid = blockIdx.x;
  const int head = (bid & 7) + 8 * (bid >> 4), half = (bid >> 3) & 1;

  const __amdgpu_buffer_rsrc_t rq = __builtin_amdgcn_make_buffer_rsrc((char*)Q + (size_t)head * (SQ * DH * 2), 0, SQ * DH * 2, 0x00020000);
  const __amdgpu_buffer_rsrc_t rk = __builtin_amdgcn_make_buffer_rsrc((char*)K + (size_t)head * (SKV * DH * 2), 0, SKV * DH * 2, 0x00020000);
  const __amdgpu_buffer_rsrc_t rv = __builtin_amdgcn_make_buffer_rsrc((char*)V + (size_t)head * (SKV * DH * 2), 0, SKV * DH * 2, 0x00020000);

  v4u qf[2][4];
#pragma unroll
  for (int h = 0; h < 2; ++h)
#pragma unroll
    for (int b = 0; b < 4; ++b) qf[h][b] = __builtin_amdgcn_raw_buffer_load_b128(rq, r * 512 + h * 256 + g * 64 + b * 16, 0, 0);

  const int klane = ((lane >> 2) & 3) * 512 + g * 64 + (lane & 3) * 16;
  const int vlane = g * 512 + half * 256 + r * 16;
  int base = w * (TK * 512);
  v4u kf[4][2], vf[4];
#pragma unroll
  for (int A = 0; A < 4; ++A)
#pragma unroll
    for (int h = 0; h < 2; ++h) kf[A][h] = __builtin_amdgcn_raw_buffer_load_b128(rk, base + klane + A * 2048 + h * 256, 0, AUXK);
#pragma unroll
  for (int i = 0; i < 4; ++i) vf[i] = __builtin_amdgcn_raw_buffer_load_b128(rv, base + vlane + i * 2048, 0, AUXV);
  __builtin_amdgcn_sched_barrier(0);

  f32x4 acc[8];
#pragma unroll
  for (int e = 0; e < 8; ++e) acc[e] = f32x4{0.f, 0.f, 0.f, 0.f};
  float mc = -__builtin_inff(), l = 0.f;

  for (int it = 0; it < NT / NW; ++it) {
    base += NW * TK * 512;
    const int kb = base + klane, vb = base + vlane;
    float s[4];
#pragma unroll
    for (int A = 0; A < 4; ++A) {
      f32x4 sa[4];
#pragma unroll
      for (int b = 0; b < 4; ++b) sa[b] = mfma32(kf[A][0], qf[0][b], f32x4{0.f, 0.f, 0.f, 0.f});
      kf[A][0] = __builtin_amdgcn_raw_buffer_load_b128(rk, kb + A * 2048, 0, AUXK);
      __builtin_amdgcn_sched_barrier(0);
#pragma unroll
      for (int b = 0; b < 4; ++b) sa[b] = mfma32(kf[A][1], qf[1][b], sa[b]);
      kf[A][1] = __builtin_amdgcn_raw_buffer_load_b128(rk, kb + A * 2048 + 256, 0, AUXK);
      __builtin_amdgcn_sched_barrier(0);
      s[A] = (sa[0][0] + sa[1][1]) + (sa[2][2] + sa[3][3]);
    }
    const float mx = xg_max(fmaxf(fmaxf(s[0], s[1]), fmaxf(s[2], s[3])));
    const float mn = fmaxf(mc, mx * C2);
    const float alpha = __builtin_amdgcn_exp2f(mc - mn);
    mc = mn;
    float p[4];
#pragma unroll
    for (int A = 0; A < 4; ++A) p[A] = __builtin_amdgcn_exp2f(__builtin_fmaf(s[A], C2, -mn));
    l = __builtin_fmaf(l, alpha, (p[0] + p[1]) + (p[2] + p[3]));
    const v2u pb = {pack_bf16(p[0], p[1]), pack_bf16(p[2], p[3])};
    if (__any(alpha != 1.f)) {
#pragma unroll
      for (int e = 0; e < 8; ++e) acc[e] *= alpha;
    }
    v2u va[8];
#pragma unroll
    for (int k = 0; k < 4; ++k) {
      va[2 * k][0] = __builtin_amdgcn_perm(vf[1][k], vf[0][k], 0x05040100u);
      va[2 * k + 1][0] = __builtin_amdgcn_perm(vf[1][k], vf[0][k], 0x07060302u);
    }
    vf[0] = __builtin_amdgcn_raw_buffer_load_b128(rv, vb, 0, AUXV);
    vf[1] = __builtin_amdgcn_raw_buffer_load_b128(rv, vb + 2048, 0, AUXV);
    __builtin_amdgcn_sched_barrier(0);
#pragma unroll
    for (int k = 0; k < 4; ++k) {
      va[2 * k][1] = __builtin_amdgcn_perm(vf[3][k], vf[2][k], 0x05040100u);
      va[2 * k + 1][1] = __builtin_amdgcn_perm(vf[3][k], vf[2][k], 0x07060302u);
    }
    vf[2] = __builtin_amdgcn_raw_buffer_load_b128(rv, vb + 4096, 0, AUXV);
    vf[3] = __builtin_amdgcn_raw_buffer_load_b128(rv, vb + 6144, 0, AUXV);
    __builtin_amdgcn_sched_barrier(0);
#pragma unroll
    for (int e = 0; e < 8; ++e) acc[e] = mfma16(va[e], pb, acc[e]);
    __builtin_amdgcn_s_barrier();
  }

  l = xg_sum(l);
  float* ob = lds + (w * SQ + r) * OSTR + 32 * g;
#pragma unroll
  for (int rho = 0; rho < 4; ++rho) {
    *(f32x4*)(ob + 8 * rho) = f32x4{acc[0][rho], acc[1][rho], acc[2][rho], acc[3][rho]};
    *(f32x4*)(ob + 8 * rho + 4) = f32x4{acc[4][rho], acc[5][rho], acc[6][rho], acc[7][rho]};
  }
  float* ml = lds + NW * SQ * OSTR;
  if (g == 0) { ml[(w * SQ + r) * 2] = mc; ml[(w * SQ + r) * 2 + 1] = l; }
  __syncthreads();
  {
    const int q = tid >> 4, c = tid & 15;
    float M = ml[q * 2];
#pragma unroll
    for (int ww = 1; ww < NW; ++ww) M = fmaxf(M, ml[(ww * SQ + q) * 2]);
    float L = 0.f;
    f32x4 o0 = {0.f, 0.f, 0.f, 0.f}, o1 = {0.f, 0.f, 0.f, 0.f};
#pragma unroll
    for (int ww = 0; ww < NW; ++ww) {
      const float f = __builtin_amdgcn_exp2f(ml[(ww * SQ + q) * 2] - M);
      L = __builtin_fmaf(f, ml[(ww * SQ + q) * 2 + 1], L);
      const float* src = lds + (ww * SQ + q) * OSTR + 8 * c;
      o0 += f * *(const f32x4*)src;
      o1 += f * *(const f32x4*)(src + 4);
    }
    const float inv = 1.f / L;
    o0 *= inv; o1 *= inv;
    v4u o = {pack_bf16(o0[0], o0[1]), pack_bf16(o0[2], o0[3]), pack_bf16(o1[0], o1[1]), pack_bf16(o1[2], o1[3])};
    *(v4u*)((char*)O + ((size_t)head * SQ + q) * (DH * 2) + half * 256 + c * 16) = o;
  }
}

extern "C" void kernel_launch(void* const* d_in, const int* in_sizes, int n_in, void* d_out, int out_size, void* d_ws,
                              size_t ws_size, hipStream_t stream) {
  attn_decode_kernel<<<2 * NHEAD, NW * 64, 0, stream>>>(d_in[0], d_in[1], d_in[2], d_out);
}
```

```cpp
#include <hip/hip_runtime.h>
#include <hip/hip_bf16.h>

typedef unsigned v4u __attribute__((ext_vector_type(4)));
typedef unsigned v2u __attribute__((ext_vector_type(2)));
typedef float f32x4 __attribute__((ext_vector_type(4)));
typedef short bf16x8 __attribute__((ext_vector_type(8)));
typedef short bf16x4 __attribute__((ext_vector_type(4)));
typedef __bf16 bf2 __attribute__((ext_vector_type(2)));

constexpr int NHEAD = 128, SQ = 16, SKV = 2048, DH = 256;
constexpr int NW = 4;
constexpr int TK = 16;
constexpr int NT = SKV / TK;
#define AUXK 2
#define AUXV 2
constexpr int OSTR = 132;
constexpr float C2 = 0.0625f * 1.4426950408889634f;

__device__ __forceinline__ f32x4 mfma32(v4u a, v4u b, f32x4 c) {
  return __builtin_amdgcn_mfma_f32_16x16x32_bf16(__builtin_bit_cast(bf16x8, a), __builtin_bit_cast(bf16x8, b), c, 0, 0, 0);
}
__device__ __forceinline__ f32x4 mfma16(v2u a, v2u b, f32x4 c) {
  return __builtin_amdgcn_mfma_f32_16x16x16bf16_1k(__builtin_bit_cast(bf16x4, a), __builtin_bit_cast(bf16x4, b), c, 0, 0, 0);
}
__device__ __forceinline__ unsigned pack_bf16(float lo, float hi) {
  bf2 r = {(__bf16)lo, (__bf16)hi};
  return __builtin_bit_cast(unsigned, r);
}
__device__ __forceinline__ float xg_max(float x) {
  auto s = __builtin_amdgcn_permlane16_swap(__float_as_uint(x), __float_as_uint(x), false, false);
  x = fmaxf(__uint_as_float(s[0]), __uint_as_float(s[1]));
  auto t = __builtin_amdgcn_permlane32_swap(__float_as_uint(x), __float_as_uint(x), false, false);
  return fmaxf(__uint_as_float(t[0]), __uint_as_float(t[1]));
}
__device__ __forceinline__ float xg_sum(float x) {
  auto s = __builtin_amdgcn_permlane16_swap(__float_as_uint(x), __float_as_uint(x), false, false);
  x = __uint_as_float(s[0]) + __uint_as_float(s[1]);
  auto t = __builtin_amdgcn_permlane32_swap(__float_as_uint(x), __float_as_uint(x), false, false);
  return __uint_as_float(t[0]) + __uint_as_float(t[1]);
}

__global__ __launch_bounds__(NW * 64, 2) void attn_decode_kernel(const void* __restrict__ Q, const void* __restrict__ K,
                                                                 const void* __restrict__ V, void* __restrict__ O
) {
  __shared__ __attribute__((aligned(16))) float lds[NW * SQ * OSTR + NW * SQ * 2];
  const int tid = threadIdx.x, lane = tid & 63, w = __builtin_amdgcn_readfirstlane(tid >> 6);
  const int g = lane >> 4, r = lane & 15;
  const int bid = blockIdx.x;
  const int head = (bid & 7) + 8 * (bid >> 4), half = (bid >> 3) & 1;

  const __amdgpu_buffer_rsrc_t rq = __builtin_amdgcn_make_buffer_rsrc((char*)Q + (size_t)head * (SQ * DH * 2), 0, SQ * DH * 2, 0x00020000);
  const __amdgpu_buffer_rsrc_t rk = __builtin_amdgcn_make_buffer_rsrc((char*)K + (size_t)head * (SKV * DH * 2), 0, SKV * DH * 2, 0x00020000);
  const __amdgpu_buffer_rsrc_t rv = __builtin_amdgcn_make_buffer_rsrc((char*)V + (size_t)head * (SKV * DH * 2), 0, SKV * DH * 2, 0x00020000);

  v4u qf[2][4];
#pragma unroll
  for (int h = 0; h < 2; ++h)
#pragma unroll
    for (int b = 0; b < 4; ++b) qf[h][b] = __builtin_amdgcn_raw_buffer_load_b128(rq, r * 512 + h * 256 + g * 64 + b * 16, 0, 0);

  const int klane = ((lane >> 2) & 3) * 512 + g * 64 + (lane & 3) * 16;
  const int vlane = g * 512 + half * 256 + r * 16;
  int base = w * (TK * 512);
  v4u kf[4][2], vf[4];
#pragma unroll
  for (int A = 0; A < 4; ++A)
#pragma unroll
    for (int h = 0; h < 2; ++h) kf[A][h] = __builtin_amdgcn_raw_buffer_load_b128(rk, base + klane + A * 2048 + h * 256, 0, AUXK);
#pragma unroll
  for (int i = 0; i < 4; ++i) vf[i] = __builtin_amdgcn_raw_buffer_load_b128(rv, base + vlane + i * 2048, 0, AUXV);
  __builtin_amdgcn_sched_barrier(0);

  f32x4 acc[8];
#pragma unroll
  for (int e = 0; e < 8; ++e) acc[e] = f32x4{0.f, 0.f, 0.f, 0.f};
  float mc = -__builtin_inff(), l = 0.f;

  for (int it = 0; it < NT / NW; ++it) {
    base += NW * TK * 512;
    const int kb = base + klane, vb = base + vlane;
    float s[4];
#pragma unroll
    for (int A = 0; A < 4; ++A) {
      f32x4 sa[4];
#pragma unroll
      for (int b = 0; b < 4; ++b) sa[b] = mfma32(kf[A][0], qf[0][b], f32x4{0.f, 0.f, 0.f, 0.f});
      kf[A][0] = __builtin_amdgcn_raw_buffer_load_b128(rk, kb + A * 2048, 0, AUXK);
      __builtin_amdgcn_sched_barrier(0);
#pragma unroll
      for (int b = 0; b < 4; ++b) sa[b] = mfma32(kf[A][1], qf[1][b], sa[b]);
      kf[A][1] = __builtin_amdgcn_raw_buffer_load_b128(rk, kb + A * 2048 + 256, 0, AUXK);
      __builtin_amdgcn_sched_barrier(0);
      s[A] = (sa[0][0] + sa[1][1]) + (sa[2][2] + sa[3][3]);
    }
    const float mx = xg_max(fmaxf(fmaxf(s[0], s[1]), fmaxf(s[2], s[3])));
    const float mn = fmaxf(mc, mx * C2);
    const float alpha = __builtin_amdgcn_exp2f(mc - mn);
    mc = mn;
    float p[4];
#pragma unroll
    for (int A = 0; A < 4; ++A) p[A] = __builtin_amdgcn_exp2f(__builtin_fmaf(s[A], C2, -mn));
    l = __builtin_fmaf(l, alpha, (p[0] + p[1]) + (p[2] + p[3]));
    const v2u pb = {pack_bf16(p[0], p[1]), pack_bf16(p[2], p[3])};
    if (__any(alpha != 1.f)) {
#pragma unroll
      for (int e = 0; e < 8; ++e) acc[e] *= alpha;
    }
    v2u va[8];
#pragma unroll
    for (int k = 0; k < 4; ++k) {
      va[2 * k][0] = __builtin_amdgcn_perm(vf[1][k], vf[0][k], 0x05040100u);
      va[2 * k + 1][0] = __builtin_amdgcn_perm(vf[1][k], vf[0][k], 0x07060302u);
    }
    vf[0] = __builtin_amdgcn_raw_buffer_load_b128(rv, vb, 0, AUXV);
    vf[1] = __builtin_amdgcn_raw_buffer_load_b128(rv, vb + 2048, 0, AUXV);
    __builtin_amdgcn_sched_barrier(0);
#pragma unroll
    for (int k = 0; k < 4; ++k) {
      va[2 * k][1] = __builtin_amdgcn_perm(vf[3][k], vf[2][k], 0x05040100u);
      va[2 * k + 1][1] = __builtin_amdgcn_perm(vf[3][k], vf[2][k], 0x07060302u);
    }
    vf[2] = __builtin_amdgcn_raw_buffer_load_b128(rv, vb + 4096, 0, AUXV);
    vf[3] = __builtin_amdgcn_raw_buffer_load_b128(rv, vb + 6144, 0, AUXV);
    __builtin_amdgcn_sched_barrier(0);
#pragma unroll
    for (int e = 0; e < 8; ++e) acc[e] = mfma16(va[e], pb, acc[e]);
    __builtin_amdgcn_s_barrier();
  }

  l = xg_sum(l);
  float* ob = lds + (w * SQ + r) * OSTR + 32 * g;
#pragma unroll
  for (int rho = 0; rho < 4; ++rho) {
    *(f32x4*)(ob + 8 * rho) = f32x4{acc[0][rho], acc[1][rho], acc[2][rho], acc[3][rho]};
    *(f32x4*)(ob + 8 * rho + 4) = f32x4{acc[4][rho], acc[5][rho], acc[6][rho], acc[7][rho]};
  }
  float* ml = lds + NW * SQ * OSTR;
  if (g == 0) { ml[(w * SQ + r) * 2] = mc; ml[(w * SQ + r) * 2 + 1] = l; }
  __syncthreads();
  {
    const int q = tid >> 4, c = tid & 15;
    float M = ml[q * 2];
#pragma unroll
    for (int ww = 1; ww < NW; ++ww) M = fmaxf(M, ml[(ww * SQ + q) * 2]);
    float L = 0.f;
    f32x4 o0 = {0.f, 0.f, 0.f, 0.f}, o1 = {0.f, 0.f, 0.f, 0.f};
#pragma unroll
    for (int ww = 0; ww < NW; ++ww) {
      const float f = __builtin_amdgcn_exp2f(ml[(ww * SQ + q) * 2] - M);
      L = __builtin_fmaf(f, ml[(ww * SQ + q) * 2 + 1], L);
      const float* src = lds + (ww * SQ + q) * OSTR + 8 * c;
      o0 += f * *(const f32x4*)src;
      o1 += f * *(const f32x4*)(src + 4);
    }
    const float inv = 1.f / L;
    o0 *= inv; o1 *= inv;
    v4u o = {pack_bf16(o0[0], o0[1]), pack_bf16(o0[2], o0[3]), pack_bf16(o1[0], o1[1]), pack_bf16(o1[2], o1[3])};
    *(v4u*)((char*)O + ((size_t)head * SQ + q) * (DH * 2) + half * 256 + c * 16) = o;
  }
}

extern "C" void kernel_launch(void* const* d_in, const int* in_sizes, int n_in, void* d_out, int out_size, void* d_ws,
                              size_t ws_size, hipStream_t stream) {
  attn_decode_kernel<<<2 * NHEAD, NW * 64, 0, stream>>>(d_in[0], d_in[1], d_in[2], d_out
);
}
```

```cpp
#define PADTO 70
#define AUXK 0
#define AUXV 0
#include <hip/hip_runtime.h>
#include <hip/hip_bf16.h>

typedef unsigned v4u __attribute__((ext_vector_type(4)));
typedef unsigned v2u __attribute__((ext_vector_type(2)));
typedef float f32x4 __attribute__((ext_vector_type(4)));
typedef short bf16x8 __attribute__((ext_vector_type(8)));
typedef short bf16x4 __attribute__((ext_vector_type(4)));
typedef __bf16 bf2 __attribute__((ext_vector_type(2)));

constexpr int NHEAD = 128, SQ = 16, SKV = 2048, DH = 256;
constexpr int NW = 4;
constexpr int TK = 16;
constexpr int NT = SKV / TK;
constexpr int OSTR = 132;
constexpr float C2 = 0.0625f * 1.4426950408889634f;

__device__ __forceinline__ f32x4 mfma32(v4u a, v4u b, f32x4 c) {
  return __builtin_amdgcn_mfma_f32_16x16x32_bf16(__builtin_bit_cast(bf16x8, a), __builtin_bit_cast(bf16x8, b), c, 0, 0, 0);
}
__device__ __forceinline__ f32x4 mfma16(v2u a, v2u b, f32x4 c) {
  return __builtin_amdgcn_mfma_f32_16x16x16bf16_1k(__builtin_bit_cast(bf16x4, a), __builtin_bit_cast(bf16x4, b), c, 0, 0, 0);
}
__device__ __forceinline__ unsigned pack_bf16(float lo, float hi) {
  bf2 r = {(__bf16)lo, (__bf16)hi};
  return __builtin_bit_cast(unsigned, r);
}
__device__ __forceinline__ float xg_max(float x) {
  auto s = __builtin_amdgcn_permlane16_swap(__float_as_uint(x), __float_as_uint(x), false, false);
  x = fmaxf(__uint_as_float(s[0]), __uint_as_float(s[1]));
  auto t = __builtin_amdgcn_permlane32_swap(__float_as_uint(x), __float_as_uint(x), false, false);
  return fmaxf(__uint_as_float(t[0]), __uint_as_float(t[1]));
}
__device__ __forceinline__ float xg_sum(float x) {
  auto s = __builtin_amdgcn_permlane16_swap(__float_as_uint(x), __float_as_uint(x), false, false);
  x = __uint_as_float(s[0]) + __uint_as_float(s[1]);
  auto t = __builtin_amdgcn_permlane32_swap(__float_as_uint(x), __float_as_uint(x), false, false);
  return __uint_as_float(t[0]) + __uint_as_float(t[1]);
}

__global__ __launch_bounds__(NW * 64, 2) void attn_decode_kernel(const void* __restrict__ Q, const void* __restrict__ K,
                                                                 const void* __restrict__ V, void* __restrict__ O
) {
  __shared__ __attribute__((aligned(16))) float lds[NW * SQ * OSTR + NW * SQ * 2];
  const unsigned long long tpad0 = __builtin_amdgcn_s_memrealtime();
  const int tid = threadIdx.x, lane = tid & 63, w = __builtin_amdgcn_readfirstlane(tid >> 6);
  const int g = lane >> 4, r = lane & 15;
  const int bid = blockIdx.x;
  const int head = (bid & 7) + 8 * (bid >> 4), half = (bid >> 3) & 1;

  const __amdgpu_buffer_rsrc_t rq = __builtin_amdgcn_make_buffer_rsrc((char*)Q + (size_t)head * (SQ * DH * 2), 0, SQ * DH * 2, 0x00020000);
  const __amdgpu_buffer_rsrc_t rk = __builtin_amdgcn_make_buffer_rsrc((char*)K + (size_t)head * (SKV * DH * 2), 0, SKV * DH * 2, 0x00020000);
  const __amdgpu_buffer_rsrc_t rv = __builtin_amdgcn_make_buffer_rsrc((char*)V + (size_t)head * (SKV * DH * 2), 0, SKV * DH * 2, 0x00020000);

  v4u qf[2][4];
#pragma unroll
  for (int h = 0; h < 2; ++h)
#pragma unroll
    for (int b = 0; b < 4; ++b) qf[h][b] = __builtin_amdgcn_raw_buffer_load_b128(rq, r * 512 + h * 256 + g * 64 + b * 16, 0, 0);

  const int klane = ((lane >> 2) & 3) * 512 + g * 64 + (lane & 3) * 16;
  const int vlane = g * 512 + half * 256 + r * 16;
  int base = w * (TK * 512);
  v4u kf[4][2], vf[4];
#pragma unroll
  for (int A = 0; A < 4; ++A)
#pragma unroll
    for (int h = 0; h < 2; ++h) kf[A][h] = __builtin_amdgcn_raw_buffer_load_b128(rk, base + klane + A * 2048 + h * 256, 0, AUXK);
#pragma unroll
  for (int i = 0; i < 4; ++i) vf[i] = __builtin_amdgcn_raw_buffer_load_b128(rv, base + vlane + i * 2048, 0, AUXV);
  __builtin_amdgcn_sched_barrier(0);

  f32x4 acc[8];
#pragma unroll
  for (int e = 0; e < 8; ++e) acc[e] = f32x4{0.f, 0.f, 0.f, 0.f};
  float mc = -__builtin_inff(), l = 0.f;

  for (int it = 0; it < NT / NW; ++it) {
    base += NW * TK * 512;
    const int kb = base + klane, vb = base + vlane;
    float s[4];
#pragma unroll
    for (int A = 0; A < 4; ++A) {
      f32x4 sa[4];
#pragma unroll
      for (int b = 0; b < 4; ++b) sa[b] = mfma32(kf[A][0], qf[0][b], f32x4{0.f, 0.f, 0.f, 0.f});
      kf[A][0] = __builtin_amdgcn_raw_buffer_load_b128(rk, kb + A * 2048, 0, AUXK);
      __builtin_amdgcn_sched_barrier(0);
#pragma unroll
      for (int b = 0; b < 4; ++b) sa[b] = mfma32(kf[A][1], qf[1][b], sa[b]);
      kf[A][1] = __builtin_amdgcn_raw_buffer_load_b128(rk, kb + A * 2048 + 256, 0, AUXK);
      __builtin_amdgcn_sched_barrier(0);
      s[A] = (sa[0][0] + sa[1][1]) + (sa[2][2] + sa[3][3]);
    }
    const float mx = xg_max(fmaxf(fmaxf(s[0], s[1]), fmaxf(s[2], s[3])));
    const float mn = fmaxf(mc, mx * C2);
    const float alpha = __builtin_amdgcn_exp2f(mc - mn);
    mc = mn;
    float p[4];
#pragma unroll
    for (int A = 0; A < 4; ++A) p[A] = __builtin_amdgcn_exp2f(__builtin_fmaf(s[A], C2, -mn));
    l = __builtin_fmaf(l, alpha, (p[0] + p[1]) + (p[2] + p[3]));
    const v2u pb = {pack_bf16(p[0], p[1]), pack_bf16(p[2], p[3])};
    if (__any(alpha != 1.f)) {
#pragma unroll
      for (int e = 0; e < 8; ++e) acc[e] *= alpha;
    }
    v2u va[8];
#pragma unroll
    for (int k = 0; k < 4; ++k) {
      va[2 * k][0] = __builtin_amdgcn_perm(vf[1][k], vf[0][k], 0x05040100u);
      va[2 * k + 1][0] = __builtin_amdgcn_perm(vf[1][k], vf[0][k], 0x07060302u);
    }
    vf[0] = __builtin_amdgcn_raw_buffer_load_b128(rv, vb, 0, AUXV);
    vf[1] = __builtin_amdgcn_raw_buffer_load_b128(rv, vb + 2048, 0, AUXV);
    __builtin_amdgcn_sched_barrier(0);
#pragma unroll
    for (int k = 0; k < 4; ++k) {
      va[2 * k][1] = __builtin_amdgcn_perm(vf[3][k], vf[2][k], 0x05040100u);
      va[2 * k + 1][1] = __builtin_amdgcn_perm(vf[3][k], vf[2][k], 0x07060302u);
    }
    vf[2] = __builtin_amdgcn_raw_buffer_load_b128(rv, vb + 4096, 0, AUXV);
    vf[3] = __builtin_amdgcn_raw_buffer_load_b128(rv, vb + 6144, 0, AUXV);
    __builtin_amdgcn_sched_barrier(0);
#pragma unroll
    for (int e = 0; e < 8; ++e) acc[e] = mfma16(va[e], pb, acc[e]);
    __builtin_amdgcn_s_barrier();
  }

  l = xg_sum(l);
  float* ob = lds + (w * SQ + r) * OSTR + 32 * g;
#pragma unroll
  for (int rho = 0; rho < 4; ++rho) {
    *(f32x4*)(ob + 8 * rho) = f32x4{acc[0][rho], acc[1][rho], acc[2][rho], acc[3][rho]};
    *(f32x4*)(ob + 8 * rho + 4) = f32x4{acc[4][rho], acc[5][rho], acc[6][rho], acc[7][rho]};
  }
  float* ml = lds + NW * SQ * OSTR;
  if (g == 0) { ml[(w * SQ + r) * 2] = mc; ml[(w * SQ + r) * 2 + 1] = l; }
  __syncthreads();
  {
    const int q = tid >> 4, c = tid & 15;
    float M = ml[q * 2];
#pragma unroll
    for (int ww = 1; ww < NW; ++ww) M = fmaxf(M, ml[(ww * SQ + q) * 2]);
    float L = 0.f;
    f32x4 o0 = {0.f, 0.f, 0.f, 0.f}, o1 = {0.f, 0.f, 0.f, 0.f};
#pragma unroll
    for (int ww = 0; ww < NW; ++ww) {
      const float f = __builtin_amdgcn_exp2f(ml[(ww * SQ + q) * 2] - M);
      L = __builtin_fmaf(f, ml[(ww * SQ + q) * 2 + 1], L);
      const float* src = lds + (ww * SQ + q) * OSTR + 8 * c;
      o0 += f * *(const f32x4*)src;
      o1 += f * *(const f32x4*)(src + 4);
    }
    const float inv = 1.f / L;
    o0 *= inv; o1 *= inv;
    v4u o = {pack_bf16(o0[0], o0[1]), pack_bf16(o0[2], o0[3]), pack_bf16(o1[0], o1[1]), pack_bf16(o1[2], o1[3])};
    *(v4u*)((char*)O + ((size_t)head * SQ + q) * (DH * 2) + half * 256 + c * 16) = o;
  }
  while (__builtin_amdgcn_s_memrealtime() - tpad0 < PADTO * 100ull) __builtin_amdgcn_s_sleep(2);
}

extern "C" void kernel_launch(void* const* d_in, const int* in_sizes, int n_in, void* d_out, int out_size, void* d_ws,
                              size_t ws_size, hipStream_t stream) {
  attn_decode_kernel<<<2 * NHEAD, NW * 64, 0, stream>>>(d_in[0], d_in[1], d_in[2], d_out
);
}
```

```cpp
#include <hip/hip_runtime.h>
#include <hip/hip_bf16.h>

typedef unsigned v4u __attribute__((ext_vector_type(4)));
typedef unsigned v2u __attribute__((ext_vector_type(2)));
typedef float f32x4 __attribute__((ext_vector_type(4)));
typedef short bf16x8 __attribute__((ext_vector_type(8)));
typedef short bf16x4 __attribute__((ext_vector_type(4)));
typedef __bf16 bf2 __attribute__((ext_vector_type(2)));

constexpr int NHEAD = 128, SQ = 16, SKV = 2048, DH = 256;
constexpr int NW = 4;
constexpr int TK = 16;
constexpr int NT = SKV / TK;
#define AUXK 2
#define AUXV 2
constexpr int OSTR = 132;
constexpr float C2 = 0.0625f * 1.4426950408889634f;

__device__ __forceinline__ f32x4 mfma32(v4u a, v4u b, f32x4 c) {
  return __builtin_amdgcn_mfma_f32_16x16x32_bf16(__builtin_bit_cast(bf16x8, a), __builtin_bit_cast(bf16x8, b), c, 0, 0, 0);
}
__device__ __forceinline__ f32x4 mfma16(v2u a, v2u b, f32x4 c) {
  return __builtin_amdgcn_mfma_f32_16x16x16bf16_1k(__builtin_bit_cast(bf16x4, a), __builtin_bit_cast(bf16x4, b), c, 0, 0, 0);
}
__device__ __forceinline__ unsigned pack_bf16(float lo, float hi) {
  bf2 r = {(__bf16)lo, (__bf16)hi};
  return __builtin_bit_cast(unsigned, r);
}
__device__ __forceinline__ float xg_max(float x) {
  auto s = __builtin_amdgcn_permlane16_swap(__float_as_uint(x), __float_as_uint(x), false, false);
  x = fmaxf(__uint_as_float(s[0]), __uint_as_float(s[1]));
  auto t = __builtin_amdgcn_permlane32_swap(__float_as_uint(x), __float_as_uint(x), false, false);
  return fmaxf(__uint_as_float(t[0]), __uint_as_float(t[1]));
}
__device__ __forceinline__ float xg_sum(float x) {
  auto s = __builtin_amdgcn_permlane16_swap(__float_as_uint(x), __float_as_uint(x), false, false);
  x = __uint_as_float(s[0]) + __uint_as_float(s[1]);
  auto t = __builtin_amdgcn_permlane32_swap(__float_as_uint(x), __float_as_uint(x), false, false);
  return __uint_as_float(t[0]) + __uint_as_float(t[1]);
}

__global__ __launch_bounds__(NW * 64, 2) void attn_decode_kernel(const void* __restrict__ Q, const void* __restrict__ K,
                                                                 const void* __restrict__ V, void* __restrict__ O
) {
  __shared__ __attribute__((aligned(16))) float lds[NW * SQ * OSTR + NW * SQ * 2];
  const int tid = threadIdx.x, lane = tid & 63, w = __builtin_amdgcn_readfirstlane(tid >> 6);
  const int g = lane >> 4, r = lane & 15;
  const int bid = blockIdx.x;
  const int head = (bid & 7) + 8 * (bid >> 4), half = (bid >> 3) & 1;

  const __amdgpu_buffer_rsrc_t rq = __builtin_amdgcn_make_buffer_rsrc((char*)Q + (size_t)head * (SQ * DH * 2), 0, SQ * DH * 2, 0x00020000);
  const __amdgpu_buffer_rsrc_t rk = __builtin_amdgcn_make_buffer_rsrc((char*)K + (size_t)head * (SKV * DH * 2), 0, SKV * DH * 2, 0x00020000);
  const __amdgpu_buffer_rsrc_t rv = __builtin_amdgcn_make_buffer_rsrc((char*)V + (size_t)head * (SKV * DH * 2), 0, SKV * DH * 2, 0x00020000);

  v4u qg[2];
#pragma unroll
  for (int i = 0; i < 2; ++i) qg[i] = __builtin_amdgcn_raw_buffer_load_b128(rq, tid * 16 + i * 4096, 0, 0);

  const int klane = ((lane >> 2) & 3) * 512 + g * 64 + (lane & 3) * 16;
  const int vlane = g * 512 + half * 256 + r * 16;
  int base = w * (TK * 512);
  v4u kf[4][2], vf[4];
#pragma unroll
  for (int A = 0; A < 4; ++A)
#pragma unroll
    for (int h = 0; h < 2; ++h) kf[A][h] = __builtin_amdgcn_raw_buffer_load_b128(rk, base + klane + A * 2048 + h * 256, 0, AUXK);
#pragma unroll
  for (int i = 0; i < 4; ++i) vf[i] = __builtin_amdgcn_raw_buffer_load_b128(rv, base + vlane + i * 2048, 0, AUXV);
  __builtin_amdgcn_sched_barrier(0);
  v4u qf[2][4];
  {
    char* ql = (char*)lds;
#pragma unroll
    for (int i = 0; i < 2; ++i) *(v4u*)(ql + ((tid >> 5) + i * 8) * 528 + (tid & 31) * 16) = qg[i];
    __syncthreads();
#pragma unroll
    for (int h = 0; h < 2; ++h)
#pragma unroll
      for (int b = 0; b < 4; ++b) qf[h][b] = *(const v4u*)(ql + r * 528 + h * 256 + g * 64 + b * 16);
  }

  f32x4 acc[8];
#pragma unroll
  for (int e = 0; e < 8; ++e) acc[e] = f32x4{0.f, 0.f, 0.f, 0.f};
  float mc = -__builtin_inff(), l = 0.f;

  for (int it = 0; it < NT / NW; ++it) {
    base += NW * TK * 512;
    const int kb = base + klane, vb = base + vlane;
    float s[4];
#pragma unroll
    for (int A = 0; A < 4; ++A) {
      f32x4 sa[4];
#pragma unroll
      for (int b = 0; b < 4; ++b) sa[b] = mfma32(kf[A][0], qf[0][b], f32x4{0.f, 0.f, 0.f, 0.f});
      kf[A][0] = __builtin_amdgcn_raw_buffer_load_b128(rk, kb + A * 2048, 0, AUXK);
      __builtin_amdgcn_sched_barrier(0);
#pragma unroll
      for (int b = 0; b < 4; ++b) sa[b] = mfma32(kf[A][1], qf[1][b], sa[b]);
      kf[A][1] = __builtin_amdgcn_raw_buffer_load_b128(rk, kb + A * 2048 + 256, 0, AUXK);
      __builtin_amdgcn_sched_barrier(0);
      s[A] = (sa[0][0] + sa[1][1]) + (sa[2][2] + sa[3][3]);
    }
    const float mx = xg_max(fmaxf(fmaxf(s[0], s[1]), fmaxf(s[2], s[3])));
    const float mn = fmaxf(mc, mx * C2);
    const float alpha = __builtin_amdgcn_exp2f(mc - mn);
    mc = mn;
    float p[4];
#pragma unroll
    for (int A = 0; A < 4; ++A) p[A] = __builtin_amdgcn_exp2f(__builtin_fmaf(s[A], C2, -mn));
    l = __builtin_fmaf(l, alpha, (p[0] + p[1]) + (p[2] + p[3]));
    const v2u pb = {pack_bf16(p[0], p[1]), pack_bf16(p[2], p[3])};
    if (__any(alpha != 1.f)) {
#pragma unroll
      for (int e = 0; e < 8; ++e) acc[e] *= alpha;
    }
    v2u va[8];
#pragma unroll
    for (int k = 0; k < 4; ++k) {
      va[2 * k][0] = __builtin_amdgcn_perm(vf[1][k], vf[0][k], 0x05040100u);
      va[2 * k + 1][0] = __builtin_amdgcn_perm(vf[1][k], vf[0][k], 0x07060302u);
    }
    vf[0] = __builtin_amdgcn_raw_buffer_load_b128(rv, vb, 0, AUXV);
    vf[1] = __builtin_amdgcn_raw_buffer_load_b128(rv, vb + 2048, 0, AUXV);
    __builtin_amdgcn_sched_barrier(0);
#pragma unroll
    for (int k = 0; k < 4; ++k) {
      va[2 * k][1] = __builtin_amdgcn_perm(vf[3][k], vf[2][k], 0x05040100u);
      va[2 * k + 1][1] = __builtin_amdgcn_perm(vf[3][k], vf[2][k], 0x07060302u);
    }
    vf[2] = __builtin_amdgcn_raw_buffer_load_b128(rv, vb + 4096, 0, AUXV);
    vf[3] = __builtin_amdgcn_raw_buffer_load_b128(rv, vb + 6144, 0, AUXV);
    __builtin_amdgcn_sched_barrier(0);
#pragma unroll
    for (int e = 0; e < 8; ++e) acc[e] = mfma16(va[e], pb, acc[e]);
    __builtin_amdgcn_s_barrier();
  }

  l = xg_sum(l);
  float* ob = lds + (w * SQ + r) * OSTR + 32 * g;
#pragma unroll
  for (int rho = 0; rho < 4; ++rho) {
    *(f32x4*)(ob + 8 * rho) = f32x4{acc[0][rho], acc[1][rho], acc[2][rho], acc[3][rho]};
    *(f32x4*)(ob + 8 * rho + 4) = f32x4{acc[4][rho], acc[5][rho], acc[6][rho], acc[7][rho]};
  }
  float* ml = lds + NW * SQ * OSTR;
  if (g == 0) { ml[(w * SQ + r) * 2] = mc; ml[(w * SQ + r) * 2 + 1] = l; }
  __syncthreads();
  {
    const int q = tid >> 4, c = tid & 15;
    float M = ml[q * 2];
#pragma unroll
    for (int ww = 1; ww < NW; ++ww) M = fmaxf(M, ml[(ww * SQ + q) * 2]);
    float L = 0.f;
    f32x4 o0 = {0.f, 0.f, 0.f, 0.f}, o1 = {0.f, 0.f, 0.f, 0.f};
#pragma unroll
    for (int ww = 0; ww < NW; ++ww) {
      const float f = __builtin_amdgcn_exp2f(ml[(ww * SQ + q) * 2] - M);
      L = __builtin_fmaf(f, ml[(ww * SQ + q) * 2 + 1], L);
      const float* src = lds + (ww * SQ + q) * OSTR + 8 * c;
      o0 += f * *(const f32x4*)src;
      o1 += f * *(const f32x4*)(src + 4);
    }
    const float inv = 1.f / L;
    o0 *= inv; o1 *= inv;
    v4u o = {pack_bf16(o0[0], o0[1]), pack_bf16(o0[2], o0[3]), pack_bf16(o1[0], o1[1]), pack_bf16(o1[2], o1[3])};
    *(v4u*)((char*)O + ((size_t)head * SQ + q) * (DH * 2) + half * 256 + c * 16) = o;
  }
}

extern "C" void kernel_launch(void* const* d_in, const int* in_sizes, int n_in, void* d_out, int out_size, void* d_ws,
                              size_t ws_size, hipStream_t stream) {
  attn_decode_kernel<<<2 * NHEAD, NW * 64, 0, stream>>>(d_in[0], d_in[1], d_in[2], d_out
);
}
```

```cpp
#include <hip/hip_runtime.h>
#include <hip/hip_bf16.h>

typedef unsigned v4u __attribute__((ext_vector_type(4)));
typedef unsigned v2u __attribute__((ext_vector_type(2)));
typedef float f32x4 __attribute__((ext_vector_type(4)));
typedef short bf16x8 __attribute__((ext_vector_type(8)));
typedef short bf16x4 __attribute__((ext_vector_type(4)));
typedef __bf16 bf2 __attribute__((ext_vector_type(2)));
#define LAS __attribute__((address_space(3)))

constexpr int NHEAD = 128, SQ = 16, SKV = 2048, DH = 256;
constexpr int NC = 3;
constexpr int NTHR = (NC + 1) * 64;
constexpr int GK = 16;
constexpr int NG = SKV / GK;
#define NSLOT 10
#define WIN 48
constexpr int SLOT_BYTES = 12288;
constexpr int OSTR = 132;
constexpr int MRG_BYTES = NC * SQ * OSTR * 4 + NC * SQ * 8;
constexpr int RING_OFF = (MRG_BYTES + 255) / 256 * 256;
constexpr int FLAG_OFF = RING_OFF + NSLOT * SLOT_BYTES;
constexpr int LDS_BYTES = FLAG_OFF + 64 + NSLOT * 4;
constexpr float C2 = 0.0625f * 1.4426950408889634f;
static_assert(LDS_BYTES <= 160 * 1024 && MRG_BYTES >= 8192, "LDS");
static_assert(WIN % 12 == 0 && WIN <= 60, "window");

__device__ __forceinline__ f32x4 mfma32(v4u a, v4u b, f32x4 c) {
  return __builtin_amdgcn_mfma_f32_16x16x32_bf16(__builtin_bit_cast(bf16x8, a), __builtin_bit_cast(bf16x8, b), c, 0, 0, 0);
}
__device__ __forceinline__ f32x4 mfma16(bf16x4 a, v2u b, f32x4 c) {
  return __builtin_amdgcn_mfma_f32_16x16x16bf16_1k(a, __builtin_bit_cast(bf16x4, b), c, 0, 0, 0);
}
__device__ __forceinline__ unsigned pack_bf16(float lo, float hi) {
  bf2 r = {(__bf16)lo, (__bf16)hi};
  return __builtin_bit_cast(unsigned, r);
}
__device__ __forceinline__ float xg_max(float x) {
  auto s = __builtin_amdgcn_permlane16_swap(__float_as_uint(x), __float_as_uint(x), false, false);
  x = fmaxf(__uint_as_float(s[0]), __uint_as_float(s[1]));
  auto t = __builtin_amdgcn_permlane32_swap(__float_as_uint(x), __float_as_uint(x), false, false);
  return fmaxf(__uint_as_float(t[0]), __uint_as_float(t[1]));
}
__device__ __forceinline__ float xg_sum(float x) {
  auto s = __builtin_amdgcn_permlane16_swap(__float_as_uint(x), __float_as_uint(x), false, false);
  x = __uint_as_float(s[0]) + __uint_as_float(s[1]);
  auto t = __builtin_amdgcn_permlane32_swap(__float_as_uint(x), __float_as_uint(x), false, false);
  return __uint_as_float(t[0]) + __uint_as_float(t[1]);
}
template <int N, bool NT>
__device__ __forceinline__ void dma(unsigned lds, unsigned voff, const char* base) {
  if constexpr (NT)
    asm volatile("s_waitcnt vmcnt(%3)\n\ts_mov_b32 m0, %0\n\ts_nop 0\n\tglobal_load_lds_dwordx4 %1, %2 nt" ::"s"(lds), "v"(voff), "s"(base), "i"(N - 1) : "memory");
  else
    asm volatile("s_waitcnt vmcnt(%3)\n\ts_mov_b32 m0, %0\n\ts_nop 0\n\tglobal_load_lds_dwordx4 %1, %2" ::"s"(lds), "v"(voff), "s"(base), "i"(N - 1) : "memory");
}

__global__ __launch_bounds__(NTHR, 2) void attn_decode_kernel(const void* __restrict__ Q, const void* __restrict__ K,
                                                              const void* __restrict__ V, void* __restrict__ O
) {
  extern __shared__ __attribute__((aligned(16))) char smem[];
  LAS char* L = (LAS char*)smem;
  volatile LAS unsigned* full = (volatile LAS unsigned*)(L + FLAG_OFF);
  volatile LAS unsigned* freew = (volatile LAS unsigned*)(L + FLAG_OFF + 64);
  const int tid = threadIdx.x, lane = tid & 63, w = __builtin_amdgcn_readfirstlane(tid >> 6);
  const int g = lane >> 4, r = lane & 15;
  const int bid = blockIdx.x;
  const int head = (bid & 7) + 8 * (bid >> 4), half = (bid >> 3) & 1;
  const unsigned lbase = (unsigned)(size_t)L;

  f32x4 acc[8];
#pragma unroll
  for (int e = 0; e < 8; ++e) acc[e] = f32x4{0.f, 0.f, 0.f, 0.f};
  float mc = -__builtin_inff(), l = 0.f;

  if (w == 0) {
    unsigned ko[8], vo[4];
#pragma unroll
    for (int i = 0; i < 8; ++i) { const int row = 2 * i + (lane >> 5); ko[i] = row * 512 + (((lane & 31) ^ row) << 4); }
#pragma unroll
    for (int i = 0; i < 4; ++i) { const int row = 4 * i + g; vo[i] = row * 512 + half * 256 + ((r ^ (2 * (row & 7))) << 4); }
    const char* qb = (const char*)Q + (size_t)head * (SQ * DH * 2);
    const char* kb = (const char*)K + (size_t)head * (SKV * DH * 2);
    const char* vb = (const char*)V + (size_t)head * (SKV * DH * 2);
#pragma unroll
    for (int i = 0; i < 8; ++i) dma<64, false>(lbase + i * 1024, ko[i], qb);
    if (lane < 16 + NSLOT) ((volatile LAS unsigned*)full)[lane] = 0u;
    constexpr int GW = WIN / 12;
    int slot = 0;
#pragma unroll 1
    for (int j = 0; j < NG; ++j) {
      if (j == GW) { asm volatile("s_waitcnt lgkmcnt(0)" ::: "memory"); __builtin_amdgcn_s_barrier(); }
      if (j >= NSLOT) {
        while (__builtin_amdgcn_readfirstlane(freew[slot]) < (unsigned)(j - NSLOT + 1)) __builtin_amdgcn_s_sleep(1);
      }
      const unsigned sb = lbase + RING_OFF + slot * SLOT_BYTES;
#pragma unroll
      for (int i = 0; i < 8; ++i) dma<WIN, true>(sb + i * 1024, ko[i], kb);
#pragma unroll
      for (int i = 0; i < 3; ++i) dma<WIN, true>(sb + 8192 + i * 1024, vo[i], vb);
      asm volatile("s_waitcnt vmcnt(%0)" ::"i"(WIN - 1) : "memory");
      if (j >= GW && lane == 0) *full = (unsigned)(j - GW + 2);
      dma<WIN, true>(sb + 8192 + 3 * 1024, vo[3], vb);
      kb += GK * 512; vb += GK * 512;
      slot = slot + 1 == NSLOT ? 0 : slot + 1;
    }
#define TAILPUB(k) asm volatile("s_waitcnt vmcnt(%0)" ::"i"(12 * (k)) : "memory"); if (lane == 0) *full = (unsigned)(NG - (k) + 1);
    if constexpr (GW > 4) { TAILPUB(4) }
    if constexpr (GW > 3) { TAILPUB(3) }
    if constexpr (GW > 2) { TAILPUB(2) }
    TAILPUB(1)
    TAILPUB(0)
#undef TAILPUB
  } else {
    __builtin_amdgcn_s_barrier();
    asm volatile("" ::: "memory");
    unsigned ka[4];
#pragma unroll
    for (int s = 0; s < 4; ++s) ka[s] = r * 512 + (((4 * s + g) ^ r) << 4);
    const int vrow = 4 * g + (r >> 2);
    const unsigned va0 = 8192 + vrow * 256 + 32 * (vrow & 7) + 8 * (r & 3);
    while (__builtin_amdgcn_readfirstlane(*full) < 1u) {}
    asm volatile("" ::: "memory");
    v4u qf[8];
    bool first = true;
    int slot = w - 1;
#pragma unroll 1
    for (int j = w - 1; j < NG; j += NC) {
      while (__builtin_amdgcn_readfirstlane(*full) < (unsigned)(j + 2)) {}
      asm volatile("" ::: "memory");
      if (first) {
#pragma unroll
        for (int s = 0; s < 8; ++s) qf[s] = *(const LAS v4u*)(L + ka[s & 3] + (s >> 2) * 256);
        first = false;
      }
      LAS char* sp = L + RING_OFF + slot * SLOT_BYTES;
      v4u kf[8];
      bf16x4 vf[8];
#pragma unroll
      for (int s = 0; s < 8; ++s) kf[s] = *(const LAS v4u*)(sp + ka[s & 3] + (s >> 2) * 256);
#pragma unroll
      for (int e = 0; e < 8; ++e) vf[e] = __builtin_amdgcn_ds_read_tr16_b64_v4i16((LAS bf16x4*)(sp + (va0 ^ (32 * e))));
      asm volatile("s_waitcnt lgkmcnt(0)" ::: "memory");
      if (lane == 0) freew[slot] = (unsigned)(j + 1);
      asm volatile("" : "+v"(kf[0]), "+v"(kf[1]), "+v"(kf[2]), "+v"(kf[3]), "+v"(kf[4]), "+v"(kf[5]), "+v"(kf[6]), "+v"(kf[7]) : : "memory");
      asm volatile("" : "+v"(vf[0]), "+v"(vf[1]), "+v"(vf[2]), "+v"(vf[3]), "+v"(vf[4]), "+v"(vf[5]), "+v"(vf[6]), "+v"(vf[7]) : : "memory");
      f32x4 s0 = {0.f, 0.f, 0.f, 0.f}, s1 = {0.f, 0.f, 0.f, 0.f};
#pragma unroll
      for (int s = 0; s < 8; s += 2) { s0 = mfma32(kf[s], qf[s], s0); s1 = mfma32(kf[s + 1], qf[s + 1], s1); }
      const f32x4 sc = s0 + s1;
      const float mx = xg_max(fmaxf(fmaxf(sc[0], sc[1]), fmaxf(sc[2], sc[3])));
      const float mn = fmaxf(mc, mx * C2);
      const float alpha = __builtin_amdgcn_exp2f(mc - mn);
      mc = mn;
      float p[4];
#pragma unroll
      for (int i = 0; i < 4; ++i) p[i] = __builtin_amdgcn_exp2f(__builtin_fmaf(sc[i], C2, -mn));
      l = __builtin_fmaf(l, alpha, (p[0] + p[1]) + (p[2] + p[3]));
      const v2u pb = {pack_bf16(p[0], p[1]), pack_bf16(p[2], p[3])};
      if (__any(alpha != 1.f)) {
#pragma unroll
        for (int e = 0; e < 8; ++e) acc[e] *= alpha;
      }
#pragma unroll
      for (int e = 0; e < 8; ++e) acc[e] = mfma16(vf[e], pb, acc[e]);
      slot += NC; if (slot >= NSLOT) slot -= NSLOT;
    }
    l = xg_sum(l);
    LAS float* ob = (LAS float*)L + ((w - 1) * SQ + r) * OSTR + 4 * g;
#pragma unroll
    for (int e = 0; e < 8; ++e) *(LAS f32x4*)(ob + 16 * e) = acc[e];
    LAS float* ml = (LAS float*)L + NC * SQ * OSTR;
    if (g == 0) { ml[((w - 1) * SQ + r) * 2] = mc; ml[((w - 1) * SQ + r) * 2 + 1] = l; }
  }
  __syncthreads();
  {
    const int q = tid >> 4, c = tid & 15;
    const LAS float* ml = (const LAS float*)L + NC * SQ * OSTR;
    float M = ml[q * 2];
#pragma unroll
    for (int ww = 1; ww < NC; ++ww) M = fmaxf(M, ml[(ww * SQ + q) * 2]);
    float Ls = 0.f;
    f32x4 o0 = {0.f, 0.f, 0.f, 0.f}, o1 = {0.f, 0.f, 0.f, 0.f};
#pragma unroll
    for (int ww = 0; ww < NC; ++ww) {
      const float f = __builtin_amdgcn_exp2f(ml[(ww * SQ + q) * 2] - M);
      Ls = __builtin_fmaf(f, ml[(ww * SQ + q) * 2 + 1], Ls);
      const LAS float* src = (const LAS float*)L + (ww * SQ + q) * OSTR + 8 * c;
      o0 += f * *(const LAS f32x4*)src;
      o1 += f * *(const LAS f32x4*)(src + 4);
    }
    const float inv = 1.f / Ls;
    o0 *= inv; o1 *= inv;
    v4u o = {pack_bf16(o0[0], o0[1]), pack_bf16(o0[2], o0[3]), pack_bf16(o1[0], o1[1]), pack_bf16(o1[2], o1[3])};
    *(v4u*)((char*)O + ((size_t)head * SQ + q) * (DH * 2) + half * 256 + c * 16) = o;
  }
}

extern "C" void kernel_launch(void* const* d_in, const int* in_sizes, int n_in, void* d_out, int out_size, void* d_ws,
                              size_t ws_size, hipStream_t stream) {
  (void)hipFuncSetAttribute((const void*)attn_decode_kernel, hipFuncAttributeMaxDynamicSharedMemorySize, LDS_BYTES);
  attn_decode_kernel<<<2 * NHEAD, NTHR, LDS_BYTES, stream>>>(d_in[0], d_in[1], d_in[2], d_out
  );
}
```

```cpp
#include <hip/hip_runtime.h>
#include <hip/hip_bf16.h>

typedef unsigned v4u __attribute__((ext_vector_type(4)));
typedef unsigned v2u __attribute__((ext_vector_type(2)));
typedef float f32x4 __attribute__((ext_vector_type(4)));
typedef short bf16x8 __attribute__((ext_vector_type(8)));
typedef short bf16x4 __attribute__((ext_vector_type(4)));
typedef __bf16 bf2 __attribute__((ext_vector_type(2)));
#define LAS __attribute__((address_space(3)))

constexpr int NHEAD = 128, SQ = 16, SKV = 2048, DH = 256;
constexpr int GK = 16;
constexpr int NG = SKV / GK;
constexpr int NC = 3;
constexpr int NTHR = (NC + 1) * 64;
constexpr int SLOT_BYTES = 12288;
constexpr float C2 = 0.0625f * 1.4426950408889634f;
constexpr int Q_OFF = 0, MRG_OFF = 8192, ML_OFF = MRG_OFF + 8192, RING_OFF = ML_OFF + 1024;
template <int NSLOT_, int WIN_, int TAU16_, int BURST_, int SLACK_, int BK_ = 0> struct Cfg {
  static constexpr int BK = BK_;
  static constexpr int NSLOT = NSLOT_;
  static constexpr int WIN = WIN_;
  static constexpr int TAU16 = TAU16_;
  static constexpr int BURST = BURST_;
  static constexpr int SLACK = SLACK_;
  static constexpr int FLAG_OFF = RING_OFF + NSLOT * SLOT_BYTES;
  static constexpr int LDS_BYTES = FLAG_OFF + 256;
  static_assert(LDS_BYTES <= 160 * 1024 && NSLOT <= 16, "LDS");
  static_assert(WIN % 12 == 0 && WIN <= 60 && 8 + 12 * BURST <= WIN && BURST >= 1 && NSLOT > WIN / 12 + NC, "window");
};

__device__ __forceinline__ f32x4 mfma32(v4u a, v4u b, f32x4 c) {
  return __builtin_amdgcn_mfma_f32_16x16x32_bf16(__builtin_bit_cast(bf16x8, a), __builtin_bit_cast(bf16x8, b), c, 0, 0, 0);
}
__device__ __forceinline__ f32x4 mfma16(bf16x4 a, v2u b, f32x4 c) {
  return __builtin_amdgcn_mfma_f32_16x16x16bf16_1k(a, __builtin_bit_cast(bf16x4, b), c, 0, 0, 0);
}
__device__ __forceinline__ unsigned pack_bf16(float lo, float hi) {
  bf2 r = {(__bf16)lo, (__bf16)hi};
  return __builtin_bit_cast(unsigned, r);
}
__device__ __forceinline__ float xg_max(float x) {
  auto s = __builtin_amdgcn_permlane16_swap(__float_as_uint(x), __float_as_uint(x), false, false);
  x = fmaxf(__uint_as_float(s[0]), __uint_as_float(s[1]));
  auto t = __builtin_amdgcn_permlane32_swap(__float_as_uint(x), __float_as_uint(x), false, false);
  return fmaxf(__uint_as_float(t[0]), __uint_as_float(t[1]));
}
__device__ __forceinline__ float xg_sum(float x) {
  auto s = __builtin_amdgcn_permlane16_swap(__float_as_uint(x), __float_as_uint(x), false, false);
  x = __uint_as_float(s[0]) + __uint_as_float(s[1]);
  auto t = __builtin_amdgcn_permlane32_swap(__float_as_uint(x), __float_as_uint(x), false, false);
  return __uint_as_float(t[0]) + __uint_as_float(t[1]);
}
template <int N>
__device__ __forceinline__ void dma4(unsigned lds, unsigned v0, unsigned v1, unsigned v2, unsigned v3, const char* base) {
  asm volatile("s_mov_b32 m0, %0\n\ts_waitcnt vmcnt(%6)\n\tglobal_load_lds_dwordx4 %1, %5 nt\n\t"
               "s_waitcnt vmcnt(%6)\n\tglobal_load_lds_dwordx4 %2, %5 offset:1024 nt\n\t"
               "s_waitcnt vmcnt(%6)\n\tglobal_load_lds_dwordx4 %3, %5 offset:2048 nt\n\t"
               "s_waitcnt vmcnt(%6)\n\tglobal_load_lds_dwordx4 %4, %5 offset:3072 nt"
               ::"s"(lds), "v"(v0), "v"(v1), "v"(v2), "v"(v3), "s"(base), "i"(N - 1) : "memory");
}
template <int N>
__device__ __forceinline__ void dma3(unsigned lds, unsigned v0, unsigned v1, unsigned v2, const char* base) {
  asm volatile("s_mov_b32 m0, %0\n\ts_waitcnt vmcnt(%5)\n\tglobal_load_lds_dwordx4 %1, %4 nt\n\t"
               "s_waitcnt vmcnt(%5)\n\tglobal_load_lds_dwordx4 %2, %4 offset:1024 nt\n\t"
               "s_waitcnt vmcnt(%5)\n\tglobal_load_lds_dwordx4 %3, %4 offset:2048 nt\n\t"
               "s_waitcnt vmcnt(%5)"
               ::"s"(lds), "v"(v0), "v"(v1), "v"(v2), "s"(base), "i"(N - 1) : "memory");
}
__device__ __forceinline__ void dma1(unsigned lds, unsigned v3, const char* base) {
  asm volatile("s_mov_b32 m0, %0\n\ts_nop 0\n\tglobal_load_lds_dwordx4 %1, %2 offset:3072 nt" ::"s"(lds), "v"(v3), "s"(base) : "memory");
}
__device__ __forceinline__ unsigned lds_word(volatile LAS unsigned* p) { return __builtin_amdgcn_readfirstlane(*p); }
__device__ __forceinline__ unsigned clk() { return (unsigned)__builtin_amdgcn_s_memrealtime(); }

template <class C>
__global__ __launch_bounds__(NTHR, 2) void attn_decode_kernel(const void* __restrict__ Q, const void* __restrict__ K,
                                                              const void* __restrict__ V, void* __restrict__ O
) {
  constexpr int NSLOT = C::NSLOT, WIN = C::WIN, GW = WIN / 12;
  extern __shared__ __attribute__((aligned(16))) char smem[];
  LAS char* L = (LAS char*)smem;
  volatile LAS unsigned* full = (volatile LAS unsigned*)(L + C::FLAG_OFF);
  volatile LAS unsigned* stage = full + 1;
  volatile LAS unsigned* freew = full + 16;
  const int tid = threadIdx.x, lane = tid & 63, w = __builtin_amdgcn_readfirstlane(tid >> 6);
  const int g = lane >> 4, r = lane & 15;
  const int bid = blockIdx.x;
  const int head = (bid & 7) + 8 * (bid >> 4), half = (bid >> 3) & 1;
  const unsigned lbase = (unsigned)(size_t)L;

  if (__builtin_expect(w == 0, 1)) {
    unsigned kq[8], vq[4];
#pragma unroll
    for (int i = 0; i < 8; ++i) { const int row = 2 * i + (lane >> 5); kq[i] = row * 512 + (((lane & 31) ^ row) << 4) - (i & 3) * 1024; }
#pragma unroll
    for (int i = 0; i < 4; ++i) { const int row = 4 * i + g; vq[i] = row * 512 + half * 256 + ((r ^ (2 * (row & 7))) << 4) - i * 1024; }
    const char* qb = (const char*)Q + (size_t)head * (SQ * DH * 2);
    const char* kb = (const char*)K + (size_t)head * (SKV * DH * 2);
    const char* vb = (const char*)V + (size_t)head * (SKV * DH * 2);
    dma4<64>(lbase + Q_OFF, kq[0], kq[1], kq[2], kq[3], qb);
    dma4<64>(lbase + Q_OFF + 4096, kq[4], kq[5], kq[6], kq[7], qb);
    if (lane < 32) full[lane] = 0u;
    int slot = 0;
    unsigned tl0 = 0, sched = 0;
    unsigned fr = 0;
#pragma unroll 1
    for (int j = 0; j < NG; ++j) {
      if (j == C::BURST) {
        asm volatile("s_waitcnt lgkmcnt(0)" ::: "memory");
        __builtin_amdgcn_s_barrier();
        asm volatile("s_waitcnt vmcnt(%0)" ::"i"(12 * (C::BURST - 1)) : "memory");
        if (lane == 0) *full = 2u;
        tl0 = clk();
      }
      if (j >= NSLOT && __builtin_amdgcn_readfirstlane(fr) < (unsigned)(j - NSLOT + 1)) {
        while (lds_word(freew + slot) < (unsigned)(j - NSLOT + 1)) __builtin_amdgcn_s_sleep(1);
      }
      const int nslot = slot + 1 == NSLOT ? 0 : slot + 1;
      if constexpr (C::TAU16 != 0) {
        if (j > C::BURST) {
          sched += C::TAU16;
          unsigned now = clk() - tl0;
          if (now * 16u > sched + C::SLACK * 16u) sched = now * 16u - C::SLACK * 16u + C::BK * 16u;
          while (now * 16u < sched) { __builtin_amdgcn_s_sleep(1); now = clk() - tl0; }
        }
      }
      fr = freew[nslot];
      const unsigned sb = lbase + RING_OFF + slot * SLOT_BYTES;
      dma4<WIN>(sb, kq[0], kq[1], kq[2], kq[3], kb);
      dma4<WIN>(sb + 4096, kq[4], kq[5], kq[6], kq[7], kb);
      dma3<WIN>(sb + 8192, vq[0], vq[1], vq[2], vb);
      if (j > GW && lane == 0) *full = (unsigned)(j - GW + 2);
      dma1(sb + 8192, vq[3], vb);
      kb += GK * 512; vb += GK * 512;
      slot = nslot;
    }
#define TAILPUB(k) asm volatile("s_waitcnt vmcnt(%0)" ::"i"(12 * (k)) : "memory"); if (lane == 0) *full = (unsigned)(NG - (k) + 1);
    if constexpr (GW > 4) { TAILPUB(4) }
    if constexpr (GW > 3) { TAILPUB(3) }
    TAILPUB(2)
    TAILPUB(1)
    TAILPUB(0)
#undef TAILPUB
    return;
  }

  f32x4 acc[8];
#pragma unroll
  for (int e = 0; e < 8; ++e) acc[e] = f32x4{0.f, 0.f, 0.f, 0.f};
  float mc = -__builtin_inff(), l = 0.f;
  unsigned ka[4];
#pragma unroll
  for (int s = 0; s < 4; ++s) ka[s] = r * 512 + (((4 * s + g) ^ r) << 4);
  const int vrow = 4 * g + (r >> 2);
  const unsigned va0 = 8192 + vrow * 256 + 32 * (vrow & 7) + 8 * (r & 3);
  __builtin_amdgcn_s_barrier();
  asm volatile("" ::: "memory");
  while (lds_word(full) < 2u) {}
  asm volatile("" ::: "memory");
  v4u qf[8];
#pragma unroll
  for (int s = 0; s < 8; ++s) qf[s] = *(const LAS v4u*)(L + Q_OFF + ka[s & 3] + (s >> 2) * 256);
  int slot = w - 1;
#pragma unroll 1
  for (int j = w - 1; j < NG; j += NC) {
    while (lds_word(full) < (unsigned)(j + 2)) {}
    asm volatile("" ::: "memory");
    LAS char* sp = L + RING_OFF + slot * SLOT_BYTES;
    v4u kf[8];
    bf16x4 vf[8];
#pragma unroll
    for (int s = 0; s < 8; ++s) kf[s] = *(const LAS v4u*)(sp + ka[s & 3] + (s >> 2) * 256);
#pragma unroll
    for (int e = 0; e < 8; ++e) vf[e] = __builtin_amdgcn_ds_read_tr16_b64_v4i16((LAS bf16x4*)(sp + (va0 ^ (32 * e))));
    __builtin_amdgcn_sched_barrier(0);
    f32x4 s0 = {0.f, 0.f, 0.f, 0.f}, s1 = s0, s2 = s0, s3 = s0;
    s0 = mfma32(kf[0], qf[0], s0); s1 = mfma32(kf[1], qf[1], s1); s2 = mfma32(kf[2], qf[2], s2); s3 = mfma32(kf[3], qf[3], s3);
    s0 = mfma32(kf[4], qf[4], s0); s1 = mfma32(kf[5], qf[5], s1); s2 = mfma32(kf[6], qf[6], s2); s3 = mfma32(kf[7], qf[7], s3);
    __builtin_amdgcn_sched_barrier(0);
    asm volatile("s_waitcnt lgkmcnt(0)" : "+v"(vf[0]), "+v"(vf[1]), "+v"(vf[2]), "+v"(vf[3]), "+v"(vf[4]), "+v"(vf[5]), "+v"(vf[6]), "+v"(vf[7]) : : "memory");
    if (lane == 0) freew[slot] = (unsigned)(j + 1);
    const f32x4 sc = (s0 + s1) + (s2 + s3);
    const float mx = xg_max(fmaxf(fmaxf(sc[0], sc[1]), fmaxf(sc[2], sc[3])));
    const float mn = fmaxf(mc, mx * C2);
    const float alpha = __builtin_amdgcn_exp2f(mc - mn);
    mc = mn;
    float p[4];
#pragma unroll
    for (int i = 0; i < 4; ++i) p[i] = __builtin_amdgcn_exp2f(__builtin_fmaf(sc[i], C2, -mn));
    l = __builtin_fmaf(l, alpha, (p[0] + p[1]) + (p[2] + p[3]));
    v2u pb = {pack_bf16(p[0], p[1]), pack_bf16(p[2], p[3])};
    asm volatile("" : "+v"(pb));
    if (__any(alpha != 1.f)) {
#pragma unroll
      for (int e = 0; e < 8; ++e) acc[e] *= alpha;
    }
#pragma unroll
    for (int e = 0; e < 8; ++e) acc[e] = mfma16(vf[e], pb, acc[e]);
    slot += NC; if (slot >= NSLOT) slot -= NSLOT;
  }

  l = xg_sum(l);
  const int pos = (w - 1 + NC - NG % NC) % NC;
  if (pos < NC - 1) {
    LAS f32x4* mrg = (LAS f32x4*)(L + (pos == 0 ? Q_OFF : MRG_OFF)) + lane;
    LAS float* mlp = (LAS float*)(L + ML_OFF) + pos * 128 + lane;
#pragma unroll
    for (int e = 0; e < 8; ++e) mrg[e * 64] = acc[e];
    mlp[0] = mc; mlp[64] = l;
    asm volatile("s_waitcnt lgkmcnt(0)" ::: "memory");
    if (lane == 0) stage[pos] = 1u;
  } else {
    while (lds_word(stage) == 0u || lds_word(stage + 1) == 0u) {}
    asm volatile("" ::: "memory");
    const LAS float* mlp = (const LAS float*)(L + ML_OFF) + lane;
    const float ma = mlp[0], la = mlp[64], mb = mlp[128], lb = mlp[192];
    f32x4 xa[8], xb[8];
#pragma unroll
    for (int e = 0; e < 8; ++e) { xa[e] = ((const LAS f32x4*)(L + Q_OFF) + lane)[e * 64]; xb[e] = ((const LAS f32x4*)(L + MRG_OFF) + lane)[e * 64]; }
    const float mn = fmaxf(mc, fmaxf(ma, mb));
    const float f0 = __builtin_amdgcn_exp2f(mc - mn), fa = __builtin_amdgcn_exp2f(ma - mn), fb = __builtin_amdgcn_exp2f(mb - mn);
    const float inv = __builtin_amdgcn_rcpf(l * f0 + la * fa + lb * fb);
    const float g0 = f0 * inv, ga = fa * inv, gb = fb * inv;
#pragma unroll
    for (int e = 0; e < 8; ++e) acc[e] = acc[e] * g0 + xa[e] * ga + xb[e] * gb;
    char* orow = (char*)O + ((size_t)head * SQ + r) * (DH * 2) + half * 256 + (g & 1) * 32 + (g >> 1) * 16;
#pragma unroll
    for (int e = 0; e < 8; e += 2) {
      const f32x4 a = acc[e], b = acc[e + 1];
      auto x = __builtin_amdgcn_permlane16_swap(pack_bf16(a[0], a[1]), pack_bf16(b[0], b[1]), false, false);
      auto y = __builtin_amdgcn_permlane16_swap(pack_bf16(a[2], a[3]), pack_bf16(b[2], b[3]), false, false);
      *(v4u*)(orow + e * 32) = v4u{x[0], y[0], x[1], y[1]};
    }
  }
}

template <class C> static void launch_cfg(void* const* d_in, void* d_out, void* d_ws, size_t ws_size, hipStream_t stream) {
  (void)hipFuncSetAttribute((const void*)attn_decode_kernel<C>, hipFuncAttributeMaxDynamicSharedMemorySize, C::LDS_BYTES);
  attn_decode_kernel<C><<<2 * NHEAD, NTHR, C::LDS_BYTES, stream>>>(d_in[0], d_in[1], d_in[2], d_out
  );
}
extern "C" void kernel_launch(void* const* d_in, const int* in_sizes, int n_in, void* d_out, int out_size, void* d_ws,
                              size_t ws_size, hipStream_t stream) {
  launch_cfg<Cfg<11, 60, 441, 3, 28, 14>>(d_in, d_out, d_ws, ws_size, stream);
}
```

```cpp
#include <hip/hip_runtime.h>
#include <hip/hip_bf16.h>

typedef unsigned v4u __attribute__((ext_vector_type(4)));
typedef unsigned v2u __attribute__((ext_vector_type(2)));
typedef float f32x4 __attribute__((ext_vector_type(4)));
typedef short bf16x8 __attribute__((ext_vector_type(8)));
typedef short bf16x4 __attribute__((ext_vector_type(4)));
typedef __bf16 bf2 __attribute__((ext_vector_type(2)));
#define LAS __attribute__((address_space(3)))

constexpr int NHEAD = 128, SQ = 16, SKV = 2048, DH = 256;
constexpr int GK = 16;
constexpr int NG = SKV / GK;
constexpr int NC = 3;
constexpr int NTHR = (NC + 1) * 64;
constexpr int SLOT_BYTES = 12288;
constexpr float C2 = 0.0625f * 1.4426950408889634f;
constexpr int Q_OFF = 0, MRG_OFF = 8192, ML_OFF = MRG_OFF + 8192, RING_OFF = ML_OFF + 1024;
template <int NSLOT_, int WIN_, int TAU16_, int BURST_, int SLACK_, int BK_ = 0> struct Cfg {
  static constexpr int BK = BK_;
  static constexpr int NSLOT = NSLOT_;
  static constexpr int WIN = WIN_;
  static constexpr int TAU16 = TAU16_;
  static constexpr int BURST = BURST_;
  static constexpr int SLACK = SLACK_;
  static constexpr int FLAG_OFF = RING_OFF + NSLOT * SLOT_BYTES;
  static constexpr int LDS_BYTES = FLAG_OFF + 256;
  static_assert(LDS_BYTES <= 160 * 1024 && NSLOT <= 16, "LDS");
  static_assert(WIN % 12 == 0 && WIN <= 60 && 8 + 12 * BURST <= WIN && BURST >= 1 && NSLOT > WIN / 12 + NC, "window");
};

__device__ __forceinline__ f32x4 mfma32(v4u a, v4u b, f32x4 c) {
  return __builtin_amdgcn_mfma_f32_16x16x32_bf16(__builtin_bit_cast(bf16x8, a), __builtin_bit_cast(bf16x8, b), c, 0, 0, 0);
}
__device__ __forceinline__ f32x4 mfma16(bf16x4 a, v2u b, f32x4 c) {
  return __builtin_amdgcn_mfma_f32_16x16x16bf16_1k(a, __builtin_bit_cast(bf16x4, b), c, 0, 0, 0);
}
__device__ __forceinline__ unsigned pack_bf16(float lo, float hi) {
  bf2 r = {(__bf16)lo, (__bf16)hi};
  return __builtin_bit_cast(unsigned, r);
}
__device__ __forceinline__ float xg_max(float x) {
  auto s = __builtin_amdgcn_permlane16_swap(__float_as_uint(x), __float_as_uint(x), false, false);
  x = fmaxf(__uint_as_float(s[0]), __uint_as_float(s[1]));
  auto t = __builtin_amdgcn_permlane32_swap(__float_as_uint(x), __float_as_uint(x), false, false);
  return fmaxf(__uint_as_float(t[0]), __uint_as_float(t[1]));
}
__device__ __forceinline__ float xg_sum(float x) {
  auto s = __builtin_amdgcn_permlane16_swap(__float_as_uint(x), __float_as_uint(x), false, false);
  x = __uint_as_float(s[0]) + __uint_as_float(s[1]);
  auto t = __builtin_amdgcn_permlane32_swap(__float_as_uint(x), __float_as_uint(x), false, false);
  return __uint_as_float(t[0]) + __uint_as_float(t[1]);
}
template <int N>
__device__ __forceinline__ void dma4(unsigned lds, unsigned v0, unsigned v1, unsigned v2, unsigned v3, const char* base) {
  asm volatile("s_mov_b32 m0, %0\n\ts_waitcnt vmcnt(%6)\n\tglobal_load_lds_dwordx4 %1, %5 nt\n\t"
               "s_waitcnt vmcnt(%6)\n\tglobal_load_lds_dwordx4 %2, %5 offset:1024 nt\n\t"
               "s_waitcnt vmcnt(%6)\n\tglobal_load_lds_dwordx4 %3, %5 offset:2048 nt\n\t"
               "s_waitcnt vmcnt(%6)\n\tglobal_load_lds_dwordx4 %4, %5 offset:3072 nt"
               ::"s"(lds), "v"(v0), "v"(v1), "v"(v2), "v"(v3), "s"(base), "i"(N - 1) : "memory");
}
template <int N>
__device__ __forceinline__ void dma4q(unsigned lds, unsigned v0, unsigned v1, unsigned v2, unsigned v3, const char* base) {
  asm volatile("s_mov_b32 m0, %0\n\ts_waitcnt vmcnt(%6)\n\tglobal_load_lds_dwordx4 %1, %5\n\t"
               "s_waitcnt vmcnt(%6)\n\tglobal_load_lds_dwordx4 %2, %5 offset:1024\n\t"
               "s_waitcnt vmcnt(%6)\n\tglobal_load_lds_dwordx4 %3, %5 offset:2048\n\t"
               "s_waitcnt vmcnt(%6)\n\tglobal_load_lds_dwordx4 %4, %5 offset:3072"
               ::"s"(lds), "v"(v0), "v"(v1), "v"(v2), "v"(v3), "s"(base), "i"(N - 1) : "memory");
}
template <int N>
__device__ __forceinline__ void dma3(unsigned lds, unsigned v0, unsigned v1, unsigned v2, const char* base) {
  asm volatile("s_mov_b32 m0, %0\n\ts_waitcnt vmcnt(%5)\n\tglobal_load_lds_dwordx4 %1, %4 nt\n\t"
               "s_waitcnt vmcnt(%5)\n\tglobal_load_lds_dwordx4 %2, %4 offset:1024 nt\n\t"
               "s_waitcnt vmcnt(%5)\n\tglobal_load_lds_dwordx4 %3, %4 offset:2048 nt\n\t"
               "s_waitcnt vmcnt(%5)"
               ::"s"(lds), "v"(v0), "v"(v1), "v"(v2), "s"(base), "i"(N - 1) : "memory");
}
__device__ __forceinline__ void dma1(unsigned lds, unsigned v3, const char* base) {
  asm volatile("s_mov_b32 m0, %0\n\ts_nop 0\n\tglobal_load_lds_dwordx4 %1, %2 offset:3072 nt" ::"s"(lds), "v"(v3), "s"(base) : "memory");
}
__device__ __forceinline__ unsigned lds_word(volatile LAS unsigned* p) { return __builtin_amdgcn_readfirstlane(*p); }
__device__ __forceinline__ unsigned clk() { return (unsigned)__builtin_amdgcn_s_memrealtime(); }

template <class C>
__global__ __launch_bounds__(NTHR, 2) void attn_decode_kernel(const void* __restrict__ Q, const void* __restrict__ K,
                                                              const void* __restrict__ V, void* __restrict__ O
) {
  constexpr int NSLOT = C::NSLOT, WIN = C::WIN, GW = WIN / 12;
  extern __shared__ __attribute__((aligned(16))) char smem[];
  LAS char* L = (LAS char*)smem;
  volatile LAS unsigned* full = (volatile LAS unsigned*)(L + C::FLAG_OFF);
  volatile LAS unsigned* stage = full + 1;
  volatile LAS unsigned* freew = full + 16;
  const int tid = threadIdx.x, lane = tid & 63, w = __builtin_amdgcn_readfirstlane(tid >> 6);
  const int g = lane >> 4, r = lane & 15;
  const int bid = blockIdx.x;
  const int head = (bid & 7) + 8 * (bid >> 4), half = (bid >> 3) & 1;
  const unsigned lbase = (unsigned)(size_t)L;

  if (__builtin_expect(w == 0, 1)) {
    unsigned kq[8], vq[4];
#pragma unroll
    for (int i = 0; i < 8; ++i) { const int row = 2 * i + (lane >> 5); kq[i] = row * 512 + (((lane & 31) ^ row) << 4) - (i & 3) * 1024; }
#pragma unroll
    for (int i = 0; i < 4; ++i) { const int row = 4 * i + g; vq[i] = row * 512 + half * 256 + ((r ^ (2 * (row & 7))) << 4) - i * 1024; }
    const char* qb = (const char*)Q + (size_t)head * (SQ * DH * 2);
    const char* kb = (const char*)K + (size_t)head * (SKV * DH * 2);
    const char* vb = (const char*)V + (size_t)head * (SKV * DH * 2);
    dma4q<64>(lbase + Q_OFF, kq[0], kq[1], kq[2], kq[3], qb);
    dma4q<64>(lbase + Q_OFF + 4096, kq[4], kq[5], kq[6], kq[7], qb);
    if (lane < 32) full[lane] = 0u;
    int slot = 0;
    unsigned tl0 = 0, sched = 0;
    unsigned fr = 0;
#pragma unroll 1
    for (int j = 0; j < NG; ++j) {
      if (j == C::BURST) {
        asm volatile("s_waitcnt lgkmcnt(0)" ::: "memory");
        __builtin_amdgcn_s_barrier();
        asm volatile("s_waitcnt vmcnt(%0)" ::"i"(12 * (C::BURST - 1)) : "memory");
        if (lane == 0) *full = 2u;
        tl0 = clk();
      }
      if (j >= NSLOT && __builtin_amdgcn_readfirstlane(fr) < (unsigned)(j - NSLOT + 1)) {
        while (lds_word(freew + slot) < (unsigned)(j - NSLOT + 1)) __builtin_amdgcn_s_sleep(1);
      }
      const int nslot = slot + 1 == NSLOT ? 0 : slot + 1;
      if constexpr (C::TAU16 != 0) {
        if (j > C::BURST) {
          sched += C::TAU16;
          unsigned now = clk() - tl0;
          if (now * 16u > sched + C::SLACK * 16u) sched = now * 16u - C::SLACK * 16u + C::BK * 16u;
          while (now * 16u < sched) { __builtin_amdgcn_s_sleep(1); now = clk() - tl0; }
        }
      }
      fr = freew[nslot];
      const unsigned sb = lbase + RING_OFF + slot * SLOT_BYTES;
      dma4<WIN>(sb, kq[0], kq[1], kq[2], kq[3], kb);
      dma4<WIN>(sb + 4096, kq[4], kq[5], kq[6], kq[7], kb);
      dma3<WIN>(sb + 8192, vq[0], vq[1], vq[2], vb);
      if (j > GW && lane == 0) *full = (unsigned)(j - GW + 2);
      dma1(sb + 8192, vq[3], vb);
      kb += GK * 512; vb += GK * 512;
      slot = nslot;
    }
#define TAILPUB(k) asm volatile("s_waitcnt vmcnt(%0)" ::"i"(12 * (k)) : "memory"); if (lane == 0) *full = (unsigned)(NG - (k) + 1);
    if constexpr (GW > 4) { TAILPUB(4) }
    if constexpr (GW > 3) { TAILPUB(3) }
    TAILPUB(2)
    TAILPUB(1)
    TAILPUB(0)
#undef TAILPUB
    return;
  }

  f32x4 acc[8];
#pragma unroll
  for (int e = 0; e < 8; ++e) acc[e] = f32x4{0.f, 0.f, 0.f, 0.f};
  float mc = -__builtin_inff(), l = 0.f;
  unsigned ka[4];
#pragma unroll
  for (int s = 0; s < 4; ++s) ka[s] = r * 512 + (((4 * s + g) ^ r) << 4);
  const int vrow = 4 * g + (r >> 2);
  const unsigned va0 = 8192 + vrow * 256 + 32 * (vrow & 7) + 8 * (r & 3);
  __builtin_amdgcn_s_barrier();
  asm volatile("" ::: "memory");
  while (lds_word(full) < 2u) {}
  asm volatile("" ::: "memory");
  v4u qf[8];
#pragma unroll
  for (int s = 0; s < 8; ++s) qf[s] = *(const LAS v4u*)(L + Q_OFF + ka[s & 3] + (s >> 2) * 256);
  int slot = w - 1;
#pragma unroll 1
  for (int j = w - 1; j < NG; j += NC) {
    while (lds_word(full) < (unsigned)(j + 2)) {}
    asm volatile("" ::: "memory");
    LAS char* sp = L + RING_OFF + slot * SLOT_BYTES;
    v4u kf[8];
    bf16x4 vf[8];
#pragma unroll
    for (int s = 0; s < 8; ++s) kf[s] = *(const LAS v4u*)(sp + ka[s & 3] + (s >> 2) * 256);
#pragma unroll
    for (int e = 0; e < 8; ++e) vf[e] = __builtin_amdgcn_ds_read_tr16_b64_v4i16((LAS bf16x4*)(sp + (va0 ^ (32 * e))));
    __builtin_amdgcn_sched_barrier(0);
    f32x4 s0 = {0.f, 0.f, 0.f, 0.f}, s1 = s0, s2 = s0, s3 = s0;
    s0 = mfma32(kf[0], qf[0], s0); s1 = mfma32(kf[1], qf[1], s1); s2 = mfma32(kf[2], qf[2], s2); s3 = mfma32(kf[3], qf[3], s3);
    s0 = mfma32(kf[4], qf[4], s0); s1 = mfma32(kf[5], qf[5], s1); s2 = mfma32(kf[6], qf[6], s2); s3 = mfma32(kf[7], qf[7], s3);
    __builtin_amdgcn_sched_barrier(0);
    asm volatile("s_waitcnt lgkmcnt(0)" : "+v"(vf[0]), "+v"(vf[1]), "+v"(vf[2]), "+v"(vf[3]), "+v"(vf[4]), "+v"(vf[5]), "+v"(vf[6]), "+v"(vf[7]) : : "memory");
    if (lane == 0) freew[slot] = (unsigned)(j + 1);
    const f32x4 sc = (s0 + s1) + (s2 + s3);
    const float mx = xg_max(fmaxf(fmaxf(sc[0], sc[1]), fmaxf(sc[2], sc[3])));
    const float mn = fmaxf(mc, mx * C2);
    const float alpha = __builtin_amdgcn_exp2f(mc - mn);
    mc = mn;
    float p[4];
#pragma unroll
    for (int i = 0; i < 4; ++i) p[i] = __builtin_amdgcn_exp2f(__builtin_fmaf(sc[i], C2, -mn));
    l = __builtin_fmaf(l, alpha, (p[0] + p[1]) + (p[2] + p[3]));
    v2u pb = {pack_bf16(p[0], p[1]), pack_bf16(p[2], p[3])};
    asm volatile("" : "+v"(pb));
    if (__any(alpha != 1.f)) {
#pragma unroll
      for (int e = 0; e < 8; ++e) acc[e] *= alpha;
    }
#pragma unroll
    for (int e = 0; e < 8; ++e) acc[e] = mfma16(vf[e], pb, acc[e]);
    slot += NC; if (slot >= NSLOT) slot -= NSLOT;
  }

  l = xg_sum(l);
  const int pos = (w - 1 + NC - NG % NC) % NC;
  if (pos < NC - 1) {
    LAS f32x4* mrg = (LAS f32x4*)(L + (pos == 0 ? Q_OFF : MRG_OFF)) + lane;
    LAS float* mlp = (LAS float*)(L + ML_OFF) + pos * 128 + lane;
#pragma unroll
    for (int e = 0; e < 8; ++e) mrg[e * 64] = acc[e];
    mlp[0] = mc; mlp[64] = l;
    asm volatile("s_waitcnt lgkmcnt(0)" ::: "memory");
    if (lane == 0) stage[pos] = 1u;
  } else {
    while (lds_word(stage) == 0u || lds_word(stage + 1) == 0u) {}
    asm volatile("" ::: "memory");
    const LAS float* mlp = (const LAS float*)(L + ML_OFF) + lane;
    const float ma = mlp[0], la = mlp[64], mb = mlp[128], lb = mlp[192];
    f32x4 xa[8], xb[8];
#pragma unroll
    for (int e = 0; e < 8; ++e) { xa[e] = ((const LAS f32x4*)(L + Q_OFF) + lane)[e * 64]; xb[e] = ((const LAS f32x4*)(L + MRG_OFF) + lane)[e * 64]; }
    const float mn = fmaxf(mc, fmaxf(ma, mb));
    const float f0 = __builtin_amdgcn_exp2f(mc - mn), fa = __builtin_amdgcn_exp2f(ma - mn), fb = __builtin_amdgcn_exp2f(mb - mn);
    const float inv = __builtin_amdgcn_rcpf(l * f0 + la * fa + lb * fb);
    const float g0 = f0 * inv, ga = fa * inv, gb = fb * inv;
#pragma unroll
    for (int e = 0; e < 8; ++e) acc[e] = acc[e] * g0 + xa[e] * ga + xb[e] * gb;
    char* orow = (char*)O + ((size_t)head * SQ + r) * (DH * 2) + half * 256 + (g & 1) * 32 + (g >> 1) * 16;
#pragma unroll
    for (int e = 0; e < 8; e += 2) {
      const f32x4 a = acc[e], b = acc[e + 1];
      auto x = __builtin_amdgcn_permlane16_swap(pack_bf16(a[0], a[1]), pack_bf16(b[0], b[1]), false, false);
      auto y = __builtin_amdgcn_permlane16_swap(pack_bf16(a[2], a[3]), pack_bf16(b[2], b[3]), false, false);
      *(v4u*)(orow + e * 32) = v4u{x[0], y[0], x[1], y[1]};
    }
  }
}

template <class C> static void launch_cfg(void* const* d_in, void* d_out, void* d_ws, size_t ws_size, hipStream_t stream) {
  (void)hipFuncSetAttribute((const void*)attn_decode_kernel<C>, hipFuncAttributeMaxDynamicSharedMemorySize, C::LDS_BYTES);
  attn_decode_kernel<C><<<2 * NHEAD, NTHR, C::LDS_BYTES, stream>>>(d_in[0], d_in[1], d_in[2], d_out
  );
}
extern "C" void kernel_launch(void* const* d_in, const int* in_sizes, int n_in, void* d_out, int out_size, void* d_ws,
                              size_t ws_size, hipStream_t stream) {
  launch_cfg<Cfg<11, 60, 441, 3, 28, 14>>(d_in, d_out, d_ws, ws_size, stream);
}
```

```cpp
#include <hip/hip_runtime.h>
#include <hip/hip_bf16.h>

typedef unsigned v4u __attribute__((ext_vector_type(4)));
typedef unsigned v2u __attribute__((ext_vector_type(2)));
typedef float f32x4 __attribute__((ext_vector_type(4)));
typedef short bf16x8 __attribute__((ext_vector_type(8)));
typedef short bf16x4 __attribute__((ext_vector_type(4)));
typedef __bf16 bf2 __attribute__((ext_vector_type(2)));
#define LAS __attribute__((address_space(3)))

constexpr int NHEAD = 128, SQ = 16, SKV = 2048, DH = 256;
constexpr int GK = 16;
constexpr int NG = SKV / GK;
constexpr int NC = 3;
constexpr int NTHR = (NC + 1) * 64;
constexpr int SLOT_BYTES = 12288;
constexpr float C2 = 0.0625f * 1.4426950408889634f;
constexpr int Q_OFF = 0, MRG_OFF = 8192, ML_OFF = MRG_OFF + 8192, RING_OFF = ML_OFF + 1024;
constexpr int NSLOT = 11;
constexpr int WIN = 60;
constexpr int GW = WIN / 12;
constexpr int BURST = 3;
constexpr int TAU16 = 441;
constexpr int SLACK = 28;
constexpr int BACKOFF = 14;
constexpr int FLAG_OFF = RING_OFF + NSLOT * SLOT_BYTES;
constexpr int LDS_BYTES = FLAG_OFF + 256;

__device__ __forceinline__ f32x4 mfma32(v4u a, v4u b, f32x4 c) {
  return __builtin_amdgcn_mfma_f32_16x16x32_bf16(__builtin_bit_cast(bf16x8, a), __builtin_bit_cast(bf16x8, b), c, 0, 0, 0);
}
__device__ __forceinline__ f32x4 mfma16(bf16x4 a, v2u b, f32x4 c) {
  return __builtin_amdgcn_mfma_f32_16x16x16bf16_1k(a, __builtin_bit_cast(bf16x4, b), c, 0, 0, 0);
}
__device__ __forceinline__ unsigned pack_bf16(float lo, float hi) {
  bf2 r = {(__bf16)lo, (__bf16)hi};
  return __builtin_bit_cast(unsigned, r);
}
__device__ __forceinline__ float xg_max(float x) {
  auto s = __builtin_amdgcn_permlane16_swap(__float_as_uint(x), __float_as_uint(x), false, false);
  x = fmaxf(__uint_as_float(s[0]), __uint_as_float(s[1]));
  auto t = __builtin_amdgcn_permlane32_swap(__float_as_uint(x), __float_as_uint(x), false, false);
  return fmaxf(__uint_as_float(t[0]), __uint_as_float(t[1]));
}
__device__ __forceinline__ float xg_sum(float x) {
  auto s = __builtin_amdgcn_permlane16_swap(__float_as_uint(x), __float_as_uint(x), false, false);
  x = __uint_as_float(s[0]) + __uint_as_float(s[1]);
  auto t = __builtin_amdgcn_permlane32_swap(__float_as_uint(x), __float_as_uint(x), false, false);
  return __uint_as_float(t[0]) + __uint_as_float(t[1]);
}
template <int N>
__device__ __forceinline__ void dma4(unsigned lds, unsigned v0, unsigned v1, unsigned v2, unsigned v3, const char* base) {
  asm volatile("s_mov_b32 m0, %0\n\ts_waitcnt vmcnt(%6)\n\tglobal_load_lds_dwordx4 %1, %5 nt\n\t"
               "s_waitcnt vmcnt(%6)\n\tglobal_load_lds_dwordx4 %2, %5 offset:1024 nt\n\t"
               "s_waitcnt vmcnt(%6)\n\tglobal_load_lds_dwordx4 %3, %5 offset:2048 nt\n\t"
               "s_waitcnt vmcnt(%6)\n\tglobal_load_lds_dwordx4 %4, %5 offset:3072 nt"
               ::"s"(lds), "v"(v0), "v"(v1), "v"(v2), "v"(v3), "s"(base), "i"(N - 1) : "memory");
}
template <int N>
__device__ __forceinline__ void dma3(unsigned lds, unsigned v0, unsigned v1, unsigned v2, const char* base) {
  asm volatile("s_mov_b32 m0, %0\n\ts_waitcnt vmcnt(%5)\n\tglobal_load_lds_dwordx4 %1, %4 nt\n\t"
               "s_waitcnt vmcnt(%5)\n\tglobal_load_lds_dwordx4 %2, %4 offset:1024 nt\n\t"
               "s_waitcnt vmcnt(%5)\n\tglobal_load_lds_dwordx4 %3, %4 offset:2048 nt\n\t"
               "s_waitcnt vmcnt(%5)"
               ::"s"(lds), "v"(v0), "v"(v1), "v"(v2), "s"(base), "i"(N - 1) : "memory");
}
__device__ __forceinline__ void dma1(unsigned lds, unsigned v3, const char* base) {
  asm volatile("s_mov_b32 m0, %0\n\ts_nop 0\n\tglobal_load_lds_dwordx4 %1, %2 offset:3072 nt" ::"s"(lds), "v"(v3), "s"(base) : "memory");
}
__device__ __forceinline__ unsigned lds_word(volatile LAS unsigned* p) { return __builtin_amdgcn_readfirstlane(*p); }
__device__ __forceinline__ unsigned clk() { return (unsigned)__builtin_amdgcn_s_memrealtime(); }

__global__ __launch_bounds__(NTHR, 2) void attn_decode_kernel(const void* __restrict__ Q, const void* __restrict__ K,
                                                              const void* __restrict__ V, void* __restrict__ O) {
  extern __shared__ __attribute__((aligned(16))) char smem[];
  LAS char* L = (LAS char*)smem;
  volatile LAS unsigned* full = (volatile LAS unsigned*)(L + FLAG_OFF);
  volatile LAS unsigned* stage = full + 1;
  volatile LAS unsigned* freew = full + 16;
  const int tid = threadIdx.x, lane = tid & 63, w = __builtin_amdgcn_readfirstlane(tid >> 6);
  const int g = lane >> 4, r = lane & 15;
  const int bid = blockIdx.x;
  const int head = (bid & 7) + 8 * (bid >> 4), half = (bid >> 3) & 1;
  const unsigned lbase = (unsigned)(size_t)L;

  if (__builtin_expect(w == 0, 1)) {
    unsigned kq[8], vq[4];
#pragma unroll
    for (int i = 0; i < 8; ++i) { const int row = 2 * i + (lane >> 5); kq[i] = row * 512 + (((lane & 31) ^ row) << 4) - (i & 3) * 1024; }
#pragma unroll
    for (int i = 0; i < 4; ++i) { const int row = 4 * i + g; vq[i] = row * 512 + half * 256 + ((r ^ (2 * (row & 7))) << 4) - i * 1024; }
    const char* qb = (const char*)Q + (size_t)head * (SQ * DH * 2);
    const char* kb = (const char*)K + (size_t)head * (SKV * DH * 2);
    const char* vb = (const char*)V + (size_t)head * (SKV * DH * 2);
    dma4<64>(lbase + Q_OFF, kq[0], kq[1], kq[2], kq[3], qb);
    dma4<64>(lbase + Q_OFF + 4096, kq[4], kq[5], kq[6], kq[7], qb);
    if (lane < 32) full[lane] = 0u;
    int slot = 0;
    unsigned tl0 = 0, sched = 0;
    unsigned fr = 0;
#pragma unroll 1
    for (int j = 0; j < NG; ++j) {
      if (j == BURST) {
        asm volatile("s_waitcnt lgkmcnt(0)" ::: "memory");
        __builtin_amdgcn_s_barrier();
        asm volatile("s_waitcnt vmcnt(%0)" ::"i"(12 * (BURST - 1)) : "memory");
        if (lane == 0) *full = 2u;
        tl0 = clk();
      }
      if (j >= NSLOT && __builtin_amdgcn_readfirstlane(fr) < (unsigned)(j - NSLOT + 1)) {
        while (lds_word(freew + slot) < (unsigned)(j - NSLOT + 1)) __builtin_amdgcn_s_sleep(1);
      }
      const int nslot = slot + 1 == NSLOT ? 0 : slot + 1;
      if (j > BURST) {
        sched += TAU16;
        unsigned now = clk() - tl0;
        if (now * 16u > sched + SLACK * 16u) sched = now * 16u - SLACK * 16u + BACKOFF * 16u;
        while (now * 16u < sched) now = clk() - tl0;
      }
      fr = freew[nslot];
      const unsigned sb = lbase + RING_OFF + slot * SLOT_BYTES;
      dma4<WIN>(sb, kq[0], kq[1], kq[2], kq[3], kb);
      dma4<WIN>(sb + 4096, kq[4], kq[5], kq[6], kq[7], kb);
      dma3<WIN>(sb + 8192, vq[0], vq[1], vq[2], vb);
      if (j > GW && lane == 0) *full = (unsigned)(j - GW + 2);
      dma1(sb + 8192, vq[3], vb);
      kb += GK * 512; vb += GK * 512;
      slot = nslot;
    }
#define TAILPUB(k) asm volatile("s_waitcnt vmcnt(%0)" ::"i"(12 * (k)) : "memory"); if (lane == 0) *full = (unsigned)(NG - (k) + 1);
    TAILPUB(4)
    TAILPUB(3)
    TAILPUB(2)
    TAILPUB(1)
    TAILPUB(0)
#undef TAILPUB
    return;
  }

  f32x4 acc[8];
#pragma unroll
  for (int e = 0; e < 8; ++e) acc[e] = f32x4{0.f, 0.f, 0.f, 0.f};
  float mc = -__builtin_inff(), l = 0.f;
  unsigned ka[4];
#pragma unroll
  for (int s = 0; s < 4; ++s) ka[s] = r * 512 + (((4 * s + g) ^ r) << 4);
  const int vrow = 4 * g + (r >> 2);
  const unsigned va0 = 8192 + vrow * 256 + 32 * (vrow & 7) + 8 * (r & 3);
  __builtin_amdgcn_s_barrier();
  asm volatile("" ::: "memory");
  while (lds_word(full) < 2u) {}
  asm volatile("" ::: "memory");
  v4u qf[8];
#pragma unroll
  for (int s = 0; s < 8; ++s) qf[s] = *(const LAS v4u*)(L + Q_OFF + ka[s & 3] + (s >> 2) * 256);
  int slot = w - 1;
#pragma unroll 1
  for (int j = w - 1; j < NG; j += NC) {
    while (lds_word(full) < (unsigned)(j + 2)) {}
    asm volatile("" ::: "memory");
    LAS char* sp = L + RING_OFF + slot * SLOT_BYTES;
    v4u kf[8];
    bf16x4 vf[8];
#pragma unroll
    for (int s = 0; s < 8; ++s) kf[s] = *(const LAS v4u*)(sp + ka[s & 3] + (s >> 2) * 256);
#pragma unroll
    for (int e = 0; e < 8; ++e) vf[e] = __builtin_amdgcn_ds_read_tr16_b64_v4i16((LAS bf16x4*)(sp + (va0 ^ (32 * e))));
    __builtin_amdgcn_sched_barrier(0);
    f32x4 s0 = {0.f, 0.f, 0.f, 0.f}, s1 = s0, s2 = s0, s3 = s0;
    s0 = mfma32(kf[0], qf[0], s0); s1 = mfma32(kf[1], qf[1], s1); s2 = mfma32(kf[2], qf[2], s2); s3 = mfma32(kf[3], qf[3], s3);
    s0 = mfma32(kf[4], qf[4], s0); s1 = mfma32(kf[5], qf[5], s1); s2 = mfma32(kf[6], qf[6], s2); s3 = mfma32(kf[7], qf[7], s3);
    __builtin_amdgcn_sched_barrier(0);
    asm volatile("s_waitcnt lgkmcnt(0)" : "+v"(vf[0]), "+v"(vf[1]), "+v"(vf[2]), "+v"(vf[3]), "+v"(vf[4]), "+v"(vf[5]), "+v"(vf[6]), "+v"(vf[7]) : : "memory");
    if (lane == 0) freew[slot] = (unsigned)(j + 1);
    const f32x4 sc = (s0 + s1) + (s2 + s3);
    const float mx = xg_max(fmaxf(fmaxf(sc[0], sc[1]), fmaxf(sc[2], sc[3])));
    const float mn = fmaxf(mc, mx * C2);
    const float alpha = __builtin_amdgcn_exp2f(mc - mn);
    mc = mn;
    float p[4];
#pragma unroll
    for (int i = 0; i < 4; ++i) p[i] = __builtin_amdgcn_exp2f(__builtin_fmaf(sc[i], C2, -mn));
    l = __builtin_fmaf(l, alpha, (p[0] + p[1]) + (p[2] + p[3]));
    v2u pb = {pack_bf16(p[0], p[1]), pack_bf16(p[2], p[3])};
    asm volatile("" : "+v"(pb));
    if (__any(alpha != 1.f)) {
#pragma unroll
      for (int e = 0; e < 8; ++e) acc[e] *= alpha;
    }
#pragma unroll
    for (int e = 0; e < 8; ++e) acc[e] = mfma16(vf[e], pb, acc[e]);
    slot += NC; if (slot >= NSLOT) slot -= NSLOT;
  }

  l = xg_sum(l);
  const int pos = (w - 1 + NC - NG % NC) % NC;
  if (pos < NC - 1) {
    LAS f32x4* mrg = (LAS f32x4*)(L + (pos == 0 ? Q_OFF : MRG_OFF)) + lane;
    LAS float* mlp = (LAS float*)(L + ML_OFF) + pos * 128 + lane;
#pragma unroll
    for (int e = 0; e < 8; ++e) mrg[e * 64] = acc[e];
    mlp[0] = mc; mlp[64] = l;
    asm volatile("s_waitcnt lgkmcnt(0)" ::: "memory");
    if (lane == 0) stage[pos] = 1u;
  } else {
    while (lds_word(stage) == 0u || lds_word(stage + 1) == 0u) {}
    asm volatile("" ::: "memory");
    const LAS float* mlp = (const LAS float*)(L + ML_OFF) + lane;
    const float ma = mlp[0], la = mlp[64], mb = mlp[128], lb = mlp[192];
    f32x4 xa[8], xb[8];
#pragma unroll
    for (int e = 0; e < 8; ++e) { xa[e] = ((const LAS f32x4*)(L + Q_OFF) + lane)[e * 64]; xb[e] = ((const LAS f32x4*)(L + MRG_OFF) + lane)[e * 64]; }
    const float mn = fmaxf(mc, fmaxf(ma, mb));
    const float f0 = __builtin_amdgcn_exp2f(mc - mn), fa = __builtin_amdgcn_exp2f(ma - mn), fb = __builtin_amdgcn_exp2f(mb - mn);
    const float inv = __builtin_amdgcn_rcpf(l * f0 + la * fa + lb * fb);
    const float g0 = f0 * inv, ga = fa * inv, gb = fb * inv;
#pragma unroll
    for (int e = 0; e < 8; ++e) acc[e] = acc[e] * g0 + xa[e] * ga + xb[e] * gb;
    char* orow = (char*)O + ((size_t)head * SQ + r) * (DH * 2) + half * 256 + (g & 1) * 32 + (g >> 1) * 16;
#pragma unroll
    for (int e = 0; e < 8; e += 2) {
      const f32x4 a = acc[e], b = acc[e + 1];
      auto x = __builtin_amdgcn_permlane16_swap(pack_bf16(a[0], a[1]), pack_bf16(b[0], b[1]), false, false);
      auto y = __builtin_amdgcn_permlane16_swap(pack_bf16(a[2], a[3]), pack_bf16(b[2], b[3]), false, false);
      *(v4u*)(orow + e * 32) = v4u{x[0], y[0], x[1], y[1]};
    }
  }
}

extern "C" void kernel_launch(void* const* d_in, const int* in_sizes, int n_in, void* d_out, int out_size, void* d_ws,
                              size_t ws_size, hipStream_t stream) {
  (void)hipFuncSetAttribute((const void*)attn_decode_kernel, hipFuncAttributeMaxDynamicSharedMemorySize, LDS_BYTES);
  attn_decode_kernel<<<2 * NHEAD, NTHR, LDS_BYTES, stream>>>(d_in[0], d_in[1], d_in[2], d_out);
}
```
